```python
import torch

def _softmax(x, axis=-1):
    return torch.softmax(x, dim=axis)
def _gelu(x):
    return torch.nn.functional.gelu(x, approximate="tanh")
def _silu(x):
    return torch.nn.functional.silu(x)
def _sigmoid(x):
    return torch.sigmoid(x)
def _layernorm(x, axis=-1, eps=1e-5):
    m = x.mean(dim=axis, keepdim=True)
    v = x.var(dim=axis, keepdim=True, unbiased=False)
    return (x - m) / torch.sqrt(v + eps)
def _scatter_add(tgt, idx, src, axis=0):
    return tgt.clone().index_add_(axis, idx, src)
def _segment_sum(src, seg, n):
    out = torch.zeros((n,) + src.shape[1:], dtype=src.dtype)
    return out.index_add_(0, seg, src)
def _pad(x, pw):
    flat = tuple(v for pair in reversed(pw) for v in pair)
    return torch.nn.functional.pad(x, flat)

kernel_name = 'standalone__ssm_conv_s4d__prompt'

def setup_inputs():

    _w = (lambda a: (lambda t: t.to(torch.bfloat16) if t.is_floating_point() else t)(torch.as_tensor(a)))
    Bsz, L, D, N = 1, 2048, 448, 256
    return {
        "u": _w((lambda *s: torch.randn(*s, dtype=torch.bfloat16) * 0.05)(Bsz, L, D)),
        "log_A_real": _w((lambda *s: torch.randn(*s, dtype=torch.bfloat16) * 6.4)(D, N) * 0.5),
        "A_imag": _w((lambda *s: torch.randn(*s, dtype=torch.bfloat16) * 0.05)(D, N) * 3.14),
        "C_real": _w((lambda *s: torch.randn(*s, dtype=torch.bfloat16) * 0.05)(D, N) / N ** 0.5),
        "C_imag": _w((lambda *s: torch.randn(*s, dtype=torch.bfloat16) * 0.05)(D, N) / N ** 0.5),
        "log_dt": _w((lambda *s: torch.randn(*s, dtype=torch.bfloat16) * 0.05)(D) * 0.5 - 4.0),
        "D_skip": _w((lambda *s: torch.randn(*s, dtype=torch.bfloat16) * 0.05)(D)),
    }

def reference(u, log_A_real, A_imag, C_real, C_imag, log_dt, D_skip):
    L = u.shape[1]
    dt = torch.exp(log_dt)
    A = -torch.exp(log_A_real) + 1j * A_imag
    C = (C_real + 1j * C_imag) * dt[:, None]
    pos = (lambda x, dt: x.to(dt))(torch.arange(L), log_dt.dtype)
    P = torch.exp((A * dt[:, None])[:, :, None] * pos[None, None, :])
    K = 2.0 * torch.einsum("dn,dnl->dl", C, P).real
    L2 = 2 * L
    u_t = (lambda x, *d: x.permute(*d))(u, 0, 2, 1)
    Kf = (lambda x, *a, **k: torch.fft.rfft(x.float(), *a, **k))(K, L2)
    Uf = (lambda x, *a, **k: torch.fft.rfft(x.float(), *a, **k))(u_t, L2)
    y = (lambda x, *a, **k: torch.fft.irfft(x, *a, **k).to(torch.bfloat16))(Uf * Kf[None, :, :], L2)[:, :, :L]
    y = (lambda x, *d: x.permute(*d))(y, 0, 2, 1)
    return y + u * D_skip[None, None, :]
```

```cpp
#include <hip/hip_runtime.h>
#include <hip/hip_bf16.h>
#include <stdint.h>

#define LL 2048
#define DD 448
#define NN 256

static __device__ __forceinline__ float bf2f(uint16_t x){ return __uint_as_float(((uint32_t)x)<<16); }
static __device__ __forceinline__ uint16_t f2bf(float f){
  uint32_t u=__float_as_uint(f); u = u + 0x7FFF + ((u>>16)&1); return (uint16_t)(u>>16);
}
static __device__ __forceinline__ float rbf(float f){ return bf2f(f2bf(f)); }

__global__ void kgen(const uint16_t* logA, const uint16_t* Aim, const uint16_t* Cr, const uint16_t* Ci,
                     const uint16_t* logdt, float* K){
  int d = blockIdx.x;
  __shared__ float ar[NN], ai[NN], cr[NN], ci[NN];
  float dt = rbf(expf(bf2f(logdt[d])));
  for (int n=threadIdx.x;n<NN;n+=blockDim.x){
    ar[n] = -rbf(expf(bf2f(logA[d*NN+n])))*dt;
    ai[n] = bf2f(Aim[d*NN+n])*dt;
    cr[n] = bf2f(Cr[d*NN+n])*dt;
    ci[n] = bf2f(Ci[d*NN+n])*dt;
  }
  __syncthreads();
  for (int l=threadIdx.x;l<LL;l+=blockDim.x){
    float p = rbf((float)l);
    float s=0.f;
    for (int n=0;n<NN;n++){
      float e = expf(ar[n]*p);
      float sn,cs; sincosf(ai[n]*p,&sn,&cs);
      s += cr[n]*(e*cs) - ci[n]*(e*sn);
    }
    K[d*LL+l]=2.f*s;
  }
}

__global__ void conv(const uint16_t* u, const float* K, const uint16_t* Dskip, uint16_t* out){
  int d = blockIdx.x;
  __shared__ float ks[LL], us[LL];
  for (int l=threadIdx.x;l<LL;l+=blockDim.x){ ks[l]=K[d*LL+l]; us[l]=bf2f(u[l*DD+d]); }
  __syncthreads();
  float dsk = bf2f(Dskip[d]);
  for (int l=threadIdx.x;l<LL;l+=blockDim.x){
    float s=0.f;
    for (int j=0;j<=l;j++) s += ks[j]*us[l-j];
    float y = rbf(s);
    float t = rbf(us[l]*dsk);
    out[l*DD+d]=f2bf(y+t);
  }
}

extern "C" void kernel_launch(void* const* d_in, const int* in_sizes, int n_in,
                              void* d_out, int out_size, void* d_ws, size_t ws_size,
                              hipStream_t stream) {
  const uint16_t* u=(const uint16_t*)d_in[0];
  float* K=(float*)d_ws;
  kgen<<<DD,256,0,stream>>>((const uint16_t*)d_in[1],(const uint16_t*)d_in[2],(const uint16_t*)d_in[3],(const uint16_t*)d_in[4],(const uint16_t*)d_in[5],K);
  conv<<<DD,256,0,stream>>>(u,K,(const uint16_t*)d_in[6],(uint16_t*)d_out);
}
```

```cpp
#include <hip/hip_runtime.h>
#include <stdint.h>

typedef _Float16 h2 __attribute__((ext_vector_type(2)));
typedef _Float16 h8 __attribute__((ext_vector_type(8)));
typedef __bf16 bf8 __attribute__((ext_vector_type(8)));
typedef float f4 __attribute__((ext_vector_type(4)));
typedef float f16v __attribute__((ext_vector_type(16)));
typedef uint32_t u4 __attribute__((ext_vector_type(4)));
typedef uint32_t u2 __attribute__((ext_vector_type(2)));
#define LDS __attribute__((address_space(3)))

constexpr int LL = 2048, DD = 448, NN = 256;
constexpr int TROW = 2064;
constexpr int OFF_TAB = 0;
constexpr int OFF_UT = 53 * TROW;
constexpr int UT_CH = (256 + 2048) * 2;
constexpr int OFF_KR = OFF_UT + 2 * UT_CH;
constexpr int KR_COPY = 4160, KR_CH = 8320;
constexpr int PART_W = 21 * 144;
constexpr int OFF_KP = OFF_KR + 8 * PART_W;
constexpr int KP_CH = 1312;
constexpr int SMEM = OFF_KP + 2 * KP_CH;
constexpr int OFF_YX = 0;
constexpr int OFF_YB = 16384;

static __device__ __forceinline__ float bf2f(uint32_t x) { return __uint_as_float(x << 16); }
static __device__ __forceinline__ uint32_t f2bf(float f) {
  uint32_t u = __float_as_uint(f);
  return (u + 0x7FFFu + ((u >> 16) & 1u)) >> 16;
}
static __device__ __forceinline__ float rbf(float f) { return bf2f(f2bf(f)); }
static __device__ __forceinline__ uint32_t pkbf(float lo, float hi) {
  typedef __bf16 b2 __attribute__((ext_vector_type(2)));
  typedef float f2 __attribute__((ext_vector_type(2)));
  f2 v = {lo, hi};
  b2 r = __builtin_convertvector(v, b2);
  return __builtin_bit_cast(uint32_t, r);
}
static __device__ __forceinline__ h2 cmul16(h2 x, h2 w) {
  h2 t = x * w.xx;
  h2 xs = {-x.y, x.x};
  return __builtin_elementwise_fma(xs, w.yy, t);
}
struct c32 { float r, i; };
static __device__ __forceinline__ c32 csq(c32 x) {
  c32 o; float t = x.i * x.i; o.r = __builtin_fmaf(x.r, x.r, -t); float p = x.r * x.i; o.i = p + p; return o;
}
static __device__ __forceinline__ h2 toh(c32 x) { h2 o; o.x = (_Float16)x.r; o.y = (_Float16)x.i; return o; }
static __device__ __forceinline__ h2 tohc(c32 x) { h2 o; o.x = (_Float16)x.r; o.y = (_Float16)(-x.i); return o; }
static __device__ __forceinline__ float exp_acc(float x) {
  const float L2E_HI = 1.44269502162933349609375f;
  const float L2E_LO = 1.925963033500011e-08f;
  float t = x * L2E_HI;
  float e = __builtin_fmaf(x, L2E_HI, -t);
  e = __builtin_fmaf(x, L2E_LO, e);
  float n = __builtin_rintf(t);
  float f = (t - n) + e;
  float r = __builtin_amdgcn_exp2f(f);
  return __builtin_amdgcn_ldexpf(r, (int)n);
}
static __device__ __forceinline__ c32 cexp_p(float ar2, float ai, float p) {
  const float I2P_HI = 0.15915493667125701904296875f;
  const float I2P_LO = 6.4206382432e-09f;
  float m = __builtin_amdgcn_exp2f(ar2 * p);
  float x = ai * p;
  float h = x * I2P_HI;
  float e = __builtin_fmaf(x, I2P_HI, -h);
  e = __builtin_fmaf(x, I2P_LO, e);
  float f = __builtin_amdgcn_fractf(h) + e;
  c32 o; o.r = m * __builtin_amdgcn_cosf(f); o.i = m * __builtin_amdgcn_sinf(f);
  return o;
}

template <int GB, int R0>
static __device__ __forceinline__ void tab_write8(uint32_t wb, h2 a0, h2 a1, h2 a2, h2 a3, h2 a4, h2 a5, h2 a6, h2 a7) {
  asm volatile(
      "s_add_u32 m0, %0, %9\n\ts_nop 0\n\t"
      "ds_write_addtid_b32 %1 offset:%10\n\tds_write_addtid_b32 %2 offset:%11\n\t"
      "ds_write_addtid_b32 %3 offset:%12\n\tds_write_addtid_b32 %4 offset:%13\n\t"
      "ds_write_addtid_b32 %5 offset:%14\n\tds_write_addtid_b32 %6 offset:%15\n\t"
      "ds_write_addtid_b32 %7 offset:%16\n\tds_write_addtid_b32 %8 offset:%17\n\t"
      :: "s"(wb), "v"(a0), "v"(a1), "v"(a2), "v"(a3), "v"(a4), "v"(a5), "v"(a6), "v"(a7),
         "i"(GB), "i"((R0 + 0) * TROW - GB), "i"((R0 + 1) * TROW - GB), "i"((R0 + 2) * TROW - GB), "i"((R0 + 3) * TROW - GB),
         "i"((R0 + 4) * TROW - GB), "i"((R0 + 5) * TROW - GB), "i"((R0 + 6) * TROW - GB), "i"((R0 + 7) * TROW - GB)
      : "memory", "scc");
}
template <int GB, int R0>
static __device__ __forceinline__ void tab_write5(uint32_t wb, h2 a0, h2 a1, h2 a2, h2 a3, h2 a4) {
  asm volatile(
      "s_add_u32 m0, %0, %6\n\ts_nop 0\n\t"
      "ds_write_addtid_b32 %1 offset:%7\n\tds_write_addtid_b32 %2 offset:%8\n\t"
      "ds_write_addtid_b32 %3 offset:%9\n\tds_write_addtid_b32 %4 offset:%10\n\t"
      "ds_write_addtid_b32 %5 offset:%11\n\t"
      :: "s"(wb), "v"(a0), "v"(a1), "v"(a2), "v"(a3), "v"(a4),
         "i"(GB), "i"((R0 + 0) * TROW - GB), "i"((R0 + 1) * TROW - GB), "i"((R0 + 2) * TROW - GB), "i"((R0 + 3) * TROW - GB),
         "i"((R0 + 4) * TROW - GB)
      : "memory", "scc");
}

static __device__ __forceinline__ int lag2idx(int j) {
  int s = (j >= 256) + (j >= 512) + (j >= 1024);
  int half = (1 << s) >> 1;
  int q = (j + half - ((s > 0) ? (((j >> s) & 1) ^ 1) : 0) ) >> s;
  int rem = j & ((1 << s) - 1);
  int fl = j >> s;
  q = fl + ((rem > half) || (rem == half && s > 0 && (fl & 1)));
  return q + 128 * s;
}

__global__ __launch_bounds__(512, 2) void ssm_s0(const uint16_t* __restrict__ u, const uint16_t* __restrict__ logA,
                                                 const uint16_t* __restrict__ Aim, const uint16_t* __restrict__ Cre,
                                                 const uint16_t* __restrict__ Cim, const uint16_t* __restrict__ logdt,
                                                 const uint16_t* __restrict__ Dsk, uint16_t* __restrict__ out,
                                                 float* __restrict__ dbg) {
  __shared__ __attribute__((aligned(16))) char smem[SMEM];
  LDS char* sm = (LDS char*)smem;
  const int b = blockIdx.x;
  const int slot = b & 7, k = b >> 3;
  const int x = (slot < 4) ? slot : (11 - slot);
  if (x == 7) return;
  const int c0 = 64 * x + 2 * k;
  const int tid = threadIdx.x, lane = tid & 63;
  const int w = __builtin_amdgcn_readfirstlane(tid >> 6);
  const int ch = w >> 2;
  const int c = c0 + ch;
  const int n = (w & 3) * 64 + lane;

  uint32_t rA = logA[c * NN + n], rI = Aim[c * NN + n], rCr = Cre[c * NN + n], rCi = Cim[c * NN + n];
  uint32_t rdt = logdt[c], rD0 = Dsk[c0], rD1 = Dsk[c0 + 1];
  uint32_t uu[4];
#pragma unroll
  for (int i = 0; i < 4; i++) uu[i] = *(const uint32_t*)(u + (size_t)(tid + 512 * i) * DD + c0);

  if (tid < 64) {
    *(LDS u4*)(sm + OFF_UT + (tid >> 5) * UT_CH + (tid & 31) * 16) = (u4){0, 0, 0, 0};
  }

  const float dt = rbf(exp_acc(bf2f(rdt)));
  const float ar = -rbf(exp_acc(bf2f(rA))) * dt;
  const float ai = bf2f(rI) * dt;
  const float ar2 = ar * 1.44269504088896340736f;
  c32 z1 = cexp_p(ar2, ai, 1.0f);
  c32 z128 = cexp_p(ar2, ai, 128.0f);
  c32 z2 = csq(z1), z4 = csq(z2), z8 = csq(z4), z16 = csq(z8), z32 = csq(z16), z64 = csq(z32);
  c32 z256 = csq(z128), z512 = csq(z256), z1024 = csq(z512);
  float cr = bf2f(rCr), ci = bf2f(rCi);
  float mx = fmaxf(fabsf(cr), fabsf(ci));
#pragma unroll
  for (int o = 1; o < 64; o <<= 1) mx = fmaxf(mx, __shfl_xor(mx, o));
  uint32_t eb = __float_as_uint(mx) >> 23;
  eb = eb < 1 ? 127 : (eb > 253 ? 253 : eb);
  const float sc = __uint_as_float((254u - eb) << 23);
  const float isc = __uint_as_float(eb << 23);
  c32 C; C.r = cr * sc; C.i = ci * sc;

  const uint32_t wb = (uint32_t)w * 256u;
  {
    h2 e0 = {(_Float16)1.0f, (_Float16)0.0f};
    h2 e1 = tohc(z8), e2 = tohc(z16), e4 = tohc(z32), e8 = tohc(z64), e16 = tohc(z128);
    h2 e3 = cmul16(e1, e2), e5 = cmul16(e1, e4), e6 = cmul16(e2, e4), e7 = cmul16(e3, e4);
    tab_write8<0, 0>(wb, e0, e1, e2, e3, e4, e5, e6, e7);
    h2 e9 = cmul16(e1, e8), e10 = cmul16(e2, e8), e11 = cmul16(e3, e8), e12 = cmul16(e4, e8), e13 = cmul16(e5, e8),
       e14 = cmul16(e6, e8), e15 = cmul16(e7, e8);
    tab_write8<0, 8>(wb, e8, e9, e10, e11, e12, e13, e14, e15);
    tab_write8<0, 16>(wb, e16, cmul16(e1, e16), cmul16(e2, e16), cmul16(e3, e16), cmul16(e4, e16), cmul16(e5, e16),
                      cmul16(e6, e16), cmul16(e7, e16));
    tab_write8<0, 24>(wb, cmul16(e8, e16), cmul16(e9, e16), cmul16(e10, e16), cmul16(e11, e16), cmul16(e12, e16),
                      cmul16(e13, e16), cmul16(e14, e16), cmul16(e15, e16));
    h2 y1 = toh(z1), y2 = toh(z2), y4 = toh(z4), y256 = toh(z256), y512 = toh(z512), y1024 = toh(z1024);
    h2 g0 = toh(C), g1 = cmul16(g0, y1), g2 = cmul16(g0, y2), g3 = cmul16(g1, y2), g4 = cmul16(g0, y4),
       g5 = cmul16(g1, y4), g6 = cmul16(g2, y4), g7 = cmul16(g3, y4);
    tab_write8<49152, 32>(wb, g0, g1, g2, g3, g4, g5, g6, g7);
    h2 a0 = cmul16(g0, y256), a2 = cmul16(g2, y256), a4 = cmul16(g4, y256), a6 = cmul16(g6, y256);
    h2 b0 = cmul16(g0, y512), b4 = cmul16(g4, y512), d0 = cmul16(a0, y512), d4 = cmul16(a4, y512);
    tab_write8<49152, 40>(wb, a0, a2, a4, a6, b0, b4, d0, d4);
    h2 f0 = cmul16(g0, y1024), f1 = cmul16(a0, y1024), f2_ = cmul16(b0, y1024), f3 = cmul16(d0, y1024),
       f4_ = cmul16(f0, y1024);
    tab_write5<49152, 48>(wb, f0, f1, f2_, f3, f4_);
  }
#pragma unroll
  for (int i = 0; i < 4; i++) {
    int l = tid + 512 * i;
    *(LDS uint16_t*)(sm + OFF_UT + (256 + l) * 2) = (uint16_t)(uu[i] & 0xFFFF);
    *(LDS uint16_t*)(sm + OFF_UT + UT_CH + (256 + l) * 2) = (uint16_t)(uu[i] >> 16);
  }
  {
    f16v acc = {0};
    const int r = lane & 31, h = lane >> 5;
    LDS char* pa = sm + OFF_TAB + r * TROW + w * 256 + h * 16;
    LDS char* pb = pa + 32 * TROW;
    asm volatile("" ::: "memory");
#pragma unroll
    for (int s = 0; s < 8; s++) {
      h8 a = *(LDS h8*)(pa + s * 32);
      h8 bb = *(LDS h8*)(pb + s * 32);
      acc = __builtin_amdgcn_mfma_f32_32x32x16_f16(a, bb, acc, 0, 0, 0);
    }
    const float ks = 2.0f * dt * isc;
    if (r < 21) {
      LDS char* pp = sm + OFF_KR + w * PART_W + r * 144 + h * 16;
#pragma unroll
      for (int g = 0; g < 4; g++) {
        f4 v = {acc[4 * g] * ks, acc[4 * g + 1] * ks, acc[4 * g + 2] * ks, acc[4 * g + 3] * ks};
        *(LDS f4*)(pp + g * 32) = v;
      }
    }
  }
  __syncthreads();
  {
    const int t = tid & 255, cch = tid >> 8;
    if (t < 168) {
      const int cc = t >> 3, g = t & 7;
      LDS char* pp = sm + OFF_KR + cch * 4 * PART_W + cc * 144 + g * 16;
      f4 v = *(LDS f4*)(pp) + *(LDS f4*)(pp + PART_W) + *(LDS f4*)(pp + 2 * PART_W) + *(LDS f4*)(pp + 3 * PART_W);
      int base, st;
      if (cc < 8) { base = cc; st = 8; }
      else if (cc < 12) { base = 256 + (cc - 8); st = 4; }
      else if (cc < 16) { base = 384 + 64 * ((cc - 12) >> 1) + ((cc - 12) & 1); st = 2; }
      else { base = 512 + 32 * (cc - 16); st = 1; }
      LDS uint16_t* kp = (LDS uint16_t*)(sm + OFF_KP + cch * KP_CH);
#pragma unroll
      for (int i = 0; i < 4; i++) {
        int idx = base + st * (4 * g + i);
        if (idx <= 640) kp[idx] = (uint16_t)f2bf(v[i]);
      }
    }
  }
  __syncthreads();
  {
    const int i = tid & 255, cch = tid >> 8;
    LDS uint16_t* kp = (LDS uint16_t*)(sm + OFF_KP + cch * KP_CH);
    uint32_t v[9];
#pragma unroll
    for (int e = 0; e < 9; e++) {
      int j = 2047 - 8 * i - e;
      uint32_t val = kp[lag2idx(j < 0 ? 0 : j)];
      v[e] = (j < 0) ? 0u : val;
    }
    u4 c0v = {v[0] | (v[1] << 16), v[2] | (v[3] << 16), v[4] | (v[5] << 16), v[6] | (v[7] << 16)};
    u4 c1v = {v[1] | (v[2] << 16), v[3] | (v[4] << 16), v[5] | (v[6] << 16), v[7] | (v[8] << 16)};
    LDS char* kr = sm + OFF_KR + cch * KR_CH;
    *(LDS u4*)(kr + i * 16) = c0v;
    *(LDS u4*)(kr + KR_COPY + i * 16) = c1v;
    if (i < 8) {
      *(LDS u4*)(kr + (i >> 2) * KR_COPY + 4096 + (i & 3) * 16) = (u4){0, 0, 0, 0};
    }
    if (dbg) {
      for (int e = 0; e < 8; e++) { int j = 2047 - 8 * i - e; dbg[(size_t)(c0 + cch) * LL + j] = bf2f(v[e]); }
    }
  }
  __syncthreads();
  if (w < 4) {
    const int cch = w >> 1, par = w & 1;
    const int bb = lane & 15, q = lane >> 4;
    LDS char* ub = sm + OFF_UT + cch * UT_CH + (256 + 32 * (-6 - par) - 16 + 16 * bb + 8 * q) * 2;
    bf8 B[32];
#pragma unroll
    for (int v = 0; v < 32; v++) B[v] = *(LDS bf8*)(ub + v * 128);
    int x0 = 2031 - 32 * par - bb + 8 * q;
    LDS char* ka = sm + OFF_KR + cch * KR_CH + ((bb & 1) ? 2 * x0 : (KR_COPY + 2 * (x0 - 1)));
    f4 acc[8];
#pragma unroll
    for (int t = 0; t < 8; t++) acc[t] = (f4){0, 0, 0, 0};
#pragma unroll
    for (int mu = 0; mu < 32; mu++) {
      LDS uint32_t* pa = (LDS uint32_t*)(ka - mu * 128);
      u4 av = {pa[0], pa[1], pa[2], pa[3]};
      bf8 A = __builtin_bit_cast(bf8, av);
#pragma unroll
      for (int t = 0; t < 8; t++) {
        if (mu <= 4 * t + 3) acc[t] = __builtin_amdgcn_mfma_f32_16x16x32_bf16(A, B[4 * t + 3 - mu], acc[t], 0, 0, 0);
      }
    }
    LDS char* yx = sm + OFF_YX;
    f4 keep[4];
    if (par) {
#pragma unroll
      for (int i = 0; i < 4; i++) { *(LDS f4*)(yx + (w * 4 + i) * 1024 + lane * 16) = acc[i]; keep[i] = acc[4 + i]; }
    } else {
#pragma unroll
      for (int i = 0; i < 4; i++) { *(LDS f4*)(yx + (w * 4 + i) * 1024 + lane * 16) = acc[4 + i]; keep[i] = acc[i]; }
    }
    asm volatile("s_waitcnt lgkmcnt(0)" ::: "memory");
    __builtin_amdgcn_s_barrier();
#pragma unroll
    for (int i = 0; i < 4; i++) {
      f4 o = *(LDS f4*)(yx + ((w ^ 1) * 4 + i) * 1024 + lane * 16);
      f4 v = keep[i] + o;
      int t = par * 4 + i;
      u2 pk = {pkbf(v[0], v[1]), pkbf(v[2], v[3])};
      *(LDS u2*)(sm + OFF_YB + cch * 4096 + (256 * t + 16 * bb + 4 * q) * 2) = pk;
    }
  } else {
    __builtin_amdgcn_s_barrier();
  }
  __syncthreads();
  {
    const float d0 = bf2f(rD0), d1 = bf2f(rD1);
#pragma unroll
    for (int i = 0; i < 4; i++) {
      int l = tid + 512 * i;
      float y0 = bf2f(*(LDS uint16_t*)(sm + OFF_YB + l * 2));
      float y1 = bf2f(*(LDS uint16_t*)(sm + OFF_YB + 4096 + l * 2));
      float u0 = bf2f(uu[i] & 0xFFFF), u1 = bf2f(uu[i] >> 16);
      float o0 = y0 + rbf(u0 * d0), o1 = y1 + rbf(u1 * d1);
      *(uint32_t*)(out + (size_t)l * DD + c0) = pkbf(o0, o1);
    }
  }
}

extern "C" void kernel_launch(void* const* d_in, const int* in_sizes, int n_in, void* d_out, int out_size,
                              void* d_ws, size_t ws_size, hipStream_t stream) {
  float* dbg = nullptr;
  ssm_s0<<<256, 512, 0, stream>>>((const uint16_t*)d_in[0], (const uint16_t*)d_in[1], (const uint16_t*)d_in[2],
                                  (const uint16_t*)d_in[3], (const uint16_t*)d_in[4], (const uint16_t*)d_in[5],
                                  (const uint16_t*)d_in[6], (uint16_t*)d_out, dbg);
}
```

```cpp
#define PADRT 1000
#include <hip/hip_runtime.h>
#include <stdint.h>

typedef _Float16 h2 __attribute__((ext_vector_type(2)));
typedef _Float16 h8 __attribute__((ext_vector_type(8)));
typedef __bf16 bf8 __attribute__((ext_vector_type(8)));
typedef float f4 __attribute__((ext_vector_type(4)));
typedef float f16v __attribute__((ext_vector_type(16)));
typedef uint32_t u4 __attribute__((ext_vector_type(4)));
typedef uint32_t u2 __attribute__((ext_vector_type(2)));
#define LDS __attribute__((address_space(3)))

#define STAMP(i)
constexpr int LL = 2048, DD = 448, NN = 256;
constexpr int TROW = 2064;
constexpr int OFF_TAB = 0;
constexpr int OFF_UT = 53 * TROW;
constexpr int UT_CH = (256 + 2048) * 2;
constexpr int OFF_KR = OFF_UT + 2 * UT_CH;
constexpr int KR_COPY = 4160, KR_CH = 8320;
constexpr int PART_W = 21 * 144;
constexpr int OFF_KP = OFF_KR + 8 * PART_W;
constexpr int KP_CH = 1312;
constexpr int SMEM = OFF_KP + 2 * KP_CH;
constexpr int OFF_YX = 0;
constexpr int OFF_YB = 16384;

static __device__ __forceinline__ float bf2f(uint32_t x) { return __uint_as_float(x << 16); }
static __device__ __forceinline__ uint32_t f2bf(float f) {
  uint32_t u = __float_as_uint(f);
  return (u + 0x7FFFu + ((u >> 16) & 1u)) >> 16;
}
static __device__ __forceinline__ float rbf(float f) { return bf2f(f2bf(f)); }
static __device__ __forceinline__ uint32_t pkbf(float lo, float hi) {
  typedef __bf16 b2 __attribute__((ext_vector_type(2)));
  typedef float f2 __attribute__((ext_vector_type(2)));
  f2 v = {lo, hi};
  b2 r = __builtin_convertvector(v, b2);
  return __builtin_bit_cast(uint32_t, r);
}
static __device__ __forceinline__ h2 cmul16(h2 x, h2 w) {
  h2 t = x * w.xx;
  h2 xs = {-x.y, x.x};
  return __builtin_elementwise_fma(xs, w.yy, t);
}
struct c32 { float r, i; };
static __device__ __forceinline__ c32 csq(c32 x) {
  c32 o; float t = x.i * x.i; o.r = __builtin_fmaf(x.r, x.r, -t); float p = x.r * x.i; o.i = p + p; return o;
}
static __device__ __forceinline__ h2 toh(c32 x) { h2 o; o.x = (_Float16)x.r; o.y = (_Float16)x.i; return o; }
static __device__ __forceinline__ h2 tohc(c32 x) { h2 o; o.x = (_Float16)x.r; o.y = (_Float16)(-x.i); return o; }
static __device__ __forceinline__ float exp_acc(float x) {
  const float L2E_HI = 1.44269502162933349609375f;
  const float L2E_LO = 1.925963033500011e-08f;
  float t = x * L2E_HI;
  float e = __builtin_fmaf(x, L2E_HI, -t);
  e = __builtin_fmaf(x, L2E_LO, e);
  float n = __builtin_rintf(t);
  float f = (t - n) + e;
  float r = __builtin_amdgcn_exp2f(f);
  return __builtin_amdgcn_ldexpf(r, (int)n);
}
static __device__ __forceinline__ c32 cexp_p(float ar2, float ai, float p) {
  const float I2P_HI = 0.15915493667125701904296875f;
  const float I2P_LO = 6.4206382432e-09f;
  float m = __builtin_amdgcn_exp2f(ar2 * p);
  float x = ai * p;
  float h = x * I2P_HI;
  float e = __builtin_fmaf(x, I2P_HI, -h);
  e = __builtin_fmaf(x, I2P_LO, e);
  float f = __builtin_amdgcn_fractf(h) + e;
  c32 o; o.r = m * __builtin_amdgcn_cosf(f); o.i = m * __builtin_amdgcn_sinf(f);
  return o;
}

template <int GB, int R0>
static __device__ __forceinline__ void tab_write8(uint32_t wb, h2 a0, h2 a1, h2 a2, h2 a3, h2 a4, h2 a5, h2 a6, h2 a7) {
  asm volatile(
      "s_add_u32 m0, %0, %9\n\ts_nop 0\n\t"
      "ds_write_addtid_b32 %1 offset:%10\n\tds_write_addtid_b32 %2 offset:%11\n\t"
      "ds_write_addtid_b32 %3 offset:%12\n\tds_write_addtid_b32 %4 offset:%13\n\t"
      "ds_write_addtid_b32 %5 offset:%14\n\tds_write_addtid_b32 %6 offset:%15\n\t"
      "ds_write_addtid_b32 %7 offset:%16\n\tds_write_addtid_b32 %8 offset:%17\n\t"
      :: "s"(wb), "v"(a0), "v"(a1), "v"(a2), "v"(a3), "v"(a4), "v"(a5), "v"(a6), "v"(a7),
         "i"(GB), "i"((R0 + 0) * TROW - GB), "i"((R0 + 1) * TROW - GB), "i"((R0 + 2) * TROW - GB), "i"((R0 + 3) * TROW - GB),
         "i"((R0 + 4) * TROW - GB), "i"((R0 + 5) * TROW - GB), "i"((R0 + 6) * TROW - GB), "i"((R0 + 7) * TROW - GB)
      : "memory", "scc");
}
template <int GB, int R0>
static __device__ __forceinline__ void tab_write5(uint32_t wb, h2 a0, h2 a1, h2 a2, h2 a3, h2 a4) {
  asm volatile(
      "s_add_u32 m0, %0, %6\n\ts_nop 0\n\t"
      "ds_write_addtid_b32 %1 offset:%7\n\tds_write_addtid_b32 %2 offset:%8\n\t"
      "ds_write_addtid_b32 %3 offset:%9\n\tds_write_addtid_b32 %4 offset:%10\n\t"
      "ds_write_addtid_b32 %5 offset:%11\n\t"
      :: "s"(wb), "v"(a0), "v"(a1), "v"(a2), "v"(a3), "v"(a4),
         "i"(GB), "i"((R0 + 0) * TROW - GB), "i"((R0 + 1) * TROW - GB), "i"((R0 + 2) * TROW - GB), "i"((R0 + 3) * TROW - GB),
         "i"((R0 + 4) * TROW - GB)
      : "memory", "scc");
}

static __device__ __forceinline__ int lag2idx(int j) {
  int s = (j >= 256) + (j >= 512) + (j >= 1024);
  int half = (1 << s) >> 1;
  int q = (j + half - ((s > 0) ? (((j >> s) & 1) ^ 1) : 0) ) >> s;
  int rem = j & ((1 << s) - 1);
  int fl = j >> s;
  q = fl + ((rem > half) || (rem == half && s > 0 && (fl & 1)));
  return q + 128 * s;
}

__global__ __launch_bounds__(512, 2) void ssm_s0(const uint16_t* __restrict__ u, const uint16_t* __restrict__ logA,
                                                 const uint16_t* __restrict__ Aim, const uint16_t* __restrict__ Cre,
                                                 const uint16_t* __restrict__ Cim, const uint16_t* __restrict__ logdt,
                                                 const uint16_t* __restrict__ Dsk, uint16_t* __restrict__ out,
                                                 float* __restrict__ dbg, unsigned long long* __restrict__ stamps) {
  unsigned long long prt0 = __builtin_amdgcn_s_memrealtime();
  __shared__ __attribute__((aligned(16))) char smem[SMEM];
  LDS char* sm = (LDS char*)smem;
  const int b = blockIdx.x;
  const int slot = b & 7, k = b >> 3;
  const int x = (slot < 4) ? slot : (11 - slot);
  if (x == 7) return;
  const int c0 = 64 * x + 2 * k;
  const int tid = threadIdx.x, lane = tid & 63;
  const int w = __builtin_amdgcn_readfirstlane(tid >> 6);
  const int ch = w >> 2;
  const int c = c0 + ch;
  const int n = (w & 3) * 64 + lane;
  STAMP(0);

  uint32_t rA = logA[c * NN + n], rI = Aim[c * NN + n], rCr = Cre[c * NN + n], rCi = Cim[c * NN + n];
  uint32_t rdt = logdt[c], rD0 = Dsk[c0], rD1 = Dsk[c0 + 1];
  uint32_t uu[4];
#pragma unroll
  for (int i = 0; i < 4; i++) uu[i] = *(const uint32_t*)(u + (size_t)(tid + 512 * i) * DD + c0);

  if (tid < 64) {
    *(LDS u4*)(sm + OFF_UT + (tid >> 5) * UT_CH + (tid & 31) * 16) = (u4){0, 0, 0, 0};
  }

  STAMP(1);
  asm volatile("" :: "v"(rA), "v"(rI), "v"(rCr), "v"(rCi), "v"(rdt));
  STAMP(2);
  const float dt = rbf(exp_acc(bf2f(rdt)));
  const float ar = -rbf(exp_acc(bf2f(rA))) * dt;
  const float ai = bf2f(rI) * dt;
  const float ar2 = ar * 1.44269504088896340736f;
  c32 z1 = cexp_p(ar2, ai, 1.0f);
  c32 z128 = cexp_p(ar2, ai, 128.0f);
  c32 z2 = csq(z1), z4 = csq(z2), z8 = csq(z4), z16 = csq(z8), z32 = csq(z16), z64 = csq(z32);
  c32 z256 = csq(z128), z512 = csq(z256), z1024 = csq(z512);
  float cr = bf2f(rCr), ci = bf2f(rCi);
  float mx = fmaxf(fabsf(cr), fabsf(ci));
#pragma unroll
  for (int o = 1; o < 64; o <<= 1) mx = fmaxf(mx, __shfl_xor(mx, o));
  uint32_t eb = __float_as_uint(mx) >> 23;
  eb = eb < 1 ? 127 : (eb > 253 ? 253 : eb);
  const float sc = __uint_as_float((254u - eb) << 23);
  const float isc = __uint_as_float(eb << 23);
  c32 C; C.r = cr * sc; C.i = ci * sc;

  const uint32_t wb = (uint32_t)w * 256u;
  {
    h2 e0 = {(_Float16)1.0f, (_Float16)0.0f};
    h2 e1 = tohc(z8), e2 = tohc(z16), e4 = tohc(z32), e8 = tohc(z64), e16 = tohc(z128);
    h2 e3 = cmul16(e1, e2), e5 = cmul16(e1, e4), e6 = cmul16(e2, e4), e7 = cmul16(e3, e4);
    tab_write8<0, 0>(wb, e0, e1, e2, e3, e4, e5, e6, e7);
    h2 e9 = cmul16(e1, e8), e10 = cmul16(e2, e8), e11 = cmul16(e3, e8), e12 = cmul16(e4, e8), e13 = cmul16(e5, e8),
       e14 = cmul16(e6, e8), e15 = cmul16(e7, e8);
    tab_write8<0, 8>(wb, e8, e9, e10, e11, e12, e13, e14, e15);
    tab_write8<0, 16>(wb, e16, cmul16(e1, e16), cmul16(e2, e16), cmul16(e3, e16), cmul16(e4, e16), cmul16(e5, e16),
                      cmul16(e6, e16), cmul16(e7, e16));
    tab_write8<0, 24>(wb, cmul16(e8, e16), cmul16(e9, e16), cmul16(e10, e16), cmul16(e11, e16), cmul16(e12, e16),
                      cmul16(e13, e16), cmul16(e14, e16), cmul16(e15, e16));
    h2 y1 = toh(z1), y2 = toh(z2), y4 = toh(z4), y256 = toh(z256), y512 = toh(z512), y1024 = toh(z1024);
    h2 g0 = toh(C), g1 = cmul16(g0, y1), g2 = cmul16(g0, y2), g3 = cmul16(g1, y2), g4 = cmul16(g0, y4),
       g5 = cmul16(g1, y4), g6 = cmul16(g2, y4), g7 = cmul16(g3, y4);
    tab_write8<49152, 32>(wb, g0, g1, g2, g3, g4, g5, g6, g7);
    h2 a0 = cmul16(g0, y256), a2 = cmul16(g2, y256), a4 = cmul16(g4, y256), a6 = cmul16(g6, y256);
    h2 b0 = cmul16(g0, y512), b4 = cmul16(g4, y512), d0 = cmul16(a0, y512), d4 = cmul16(a4, y512);
    tab_write8<49152, 40>(wb, a0, a2, a4, a6, b0, b4, d0, d4);
    h2 f0 = cmul16(g0, y1024), f1 = cmul16(a0, y1024), f2_ = cmul16(b0, y1024), f3 = cmul16(d0, y1024),
       f4_ = cmul16(f0, y1024);
    tab_write5<49152, 48>(wb, f0, f1, f2_, f3, f4_);
  }
  STAMP(3);
#pragma unroll
  for (int i = 0; i < 4; i++) {
    int l = tid + 512 * i;
    *(LDS uint16_t*)(sm + OFF_UT + (256 + l) * 2) = (uint16_t)(uu[i] & 0xFFFF);
    *(LDS uint16_t*)(sm + OFF_UT + UT_CH + (256 + l) * 2) = (uint16_t)(uu[i] >> 16);
  }
  STAMP(4);
  {
    f16v acc = {0};
    const int r = lane & 31, h = lane >> 5;
    LDS char* pa = sm + OFF_TAB + r * TROW + w * 256 + h * 16;
    LDS char* pb = pa + 32 * TROW;
    asm volatile("" ::: "memory");
#pragma unroll
    for (int s = 0; s < 8; s++) {
      h8 a = *(LDS h8*)(pa + s * 32);
      h8 bb = *(LDS h8*)(pb + s * 32);
      acc = __builtin_amdgcn_mfma_f32_32x32x16_f16(a, bb, acc, 0, 0, 0);
    }
    const float ks = 2.0f * dt * isc;
    if (r < 21) {
      LDS char* pp = sm + OFF_KR + w * PART_W + r * 144 + h * 16;
#pragma unroll
      for (int g = 0; g < 4; g++) {
        f4 v = {acc[4 * g] * ks, acc[4 * g + 1] * ks, acc[4 * g + 2] * ks, acc[4 * g + 3] * ks};
        *(LDS f4*)(pp + g * 32) = v;
      }
    }
  }
  STAMP(5);
  __syncthreads();
  STAMP(6);
  {
    const int t = tid & 255, cch = tid >> 8;
    if (t < 168) {
      const int cc = t >> 3, g = t & 7;
      LDS char* pp = sm + OFF_KR + cch * 4 * PART_W + cc * 144 + g * 16;
      f4 v = *(LDS f4*)(pp) + *(LDS f4*)(pp + PART_W) + *(LDS f4*)(pp + 2 * PART_W) + *(LDS f4*)(pp + 3 * PART_W);
      int base, st;
      if (cc < 8) { base = cc; st = 8; }
      else if (cc < 12) { base = 256 + (cc - 8); st = 4; }
      else if (cc < 16) { base = 384 + 64 * ((cc - 12) >> 1) + ((cc - 12) & 1); st = 2; }
      else { base = 512 + 32 * (cc - 16); st = 1; }
      LDS uint16_t* kp = (LDS uint16_t*)(sm + OFF_KP + cch * KP_CH);
#pragma unroll
      for (int i = 0; i < 4; i++) {
        int idx = base + st * (4 * g + i);
        if (idx <= 640) kp[idx] = (uint16_t)f2bf(v[i]);
      }
    }
  }
  __syncthreads();
  STAMP(7);
  {
    const int i = tid & 255, cch = tid >> 8;
    LDS uint16_t* kp = (LDS uint16_t*)(sm + OFF_KP + cch * KP_CH);
    uint32_t v[9];
#pragma unroll
    for (int e = 0; e < 9; e++) {
      int j = 2047 - 8 * i - e;
      uint32_t val = kp[lag2idx(j < 0 ? 0 : j)];
      v[e] = (j < 0) ? 0u : val;
    }
    u4 c0v = {v[0] | (v[1] << 16), v[2] | (v[3] << 16), v[4] | (v[5] << 16), v[6] | (v[7] << 16)};
    u4 c1v = {v[1] | (v[2] << 16), v[3] | (v[4] << 16), v[5] | (v[6] << 16), v[7] | (v[8] << 16)};
    LDS char* kr = sm + OFF_KR + cch * KR_CH;
    *(LDS u4*)(kr + i * 16) = c0v;
    *(LDS u4*)(kr + KR_COPY + i * 16) = c1v;
    if (i < 8) {
      *(LDS u4*)(kr + (i >> 2) * KR_COPY + 4096 + (i & 3) * 16) = (u4){0, 0, 0, 0};
    }
    if (dbg) {
      for (int e = 0; e < 8; e++) { int j = 2047 - 8 * i - e; dbg[(size_t)(c0 + cch) * LL + j] = bf2f(v[e]); }
    }
  }
  __syncthreads();
  STAMP(8);
  if (w < 4) {
    const int cch = w >> 1, par = w & 1;
    const int bb = lane & 15, q = lane >> 4;
    LDS char* ub = sm + OFF_UT + cch * UT_CH + (256 + 32 * (-6 - par) - 16 + 16 * bb + 8 * q) * 2;
    bf8 B[32];
#pragma unroll
    for (int v = 0; v < 32; v++) B[v] = *(LDS bf8*)(ub + v * 128);
    int x0 = 2031 - 32 * par - bb + 8 * q;
    LDS char* ka = sm + OFF_KR + cch * KR_CH + ((bb & 1) ? 2 * x0 : (KR_COPY + 2 * (x0 - 1)));
    f4 acc[8];
#pragma unroll
    for (int t = 0; t < 8; t++) acc[t] = (f4){0, 0, 0, 0};
#pragma unroll
    for (int mu = 0; mu < 32; mu++) {
      LDS uint32_t* pa = (LDS uint32_t*)(ka - mu * 128);
      u4 av = {pa[0], pa[1], pa[2], pa[3]};
      bf8 A = __builtin_bit_cast(bf8, av);
#pragma unroll
      for (int t = 0; t < 8; t++) {
        if (mu <= 4 * t + 3) acc[t] = __builtin_amdgcn_mfma_f32_16x16x32_bf16(A, B[4 * t + 3 - mu], acc[t], 0, 0, 0);
      }
    }
    STAMP(9);
    LDS char* yx = sm + OFF_YX;
    f4 keep[4];
    if (par) {
#pragma unroll
      for (int i = 0; i < 4; i++) { *(LDS f4*)(yx + (w * 4 + i) * 1024 + lane * 16) = acc[i]; keep[i] = acc[4 + i]; }
    } else {
#pragma unroll
      for (int i = 0; i < 4; i++) { *(LDS f4*)(yx + (w * 4 + i) * 1024 + lane * 16) = acc[4 + i]; keep[i] = acc[i]; }
    }
    asm volatile("s_waitcnt lgkmcnt(0)" ::: "memory");
    __builtin_amdgcn_s_barrier();
#pragma unroll
    for (int i = 0; i < 4; i++) {
      f4 o = *(LDS f4*)(yx + ((w ^ 1) * 4 + i) * 1024 + lane * 16);
      f4 v = keep[i] + o;
      int t = par * 4 + i;
      u2 pk = {pkbf(v[0], v[1]), pkbf(v[2], v[3])};
      *(LDS u2*)(sm + OFF_YB + cch * 4096 + (256 * t + 16 * bb + 4 * q) * 2) = pk;
    }
  } else {
    __builtin_amdgcn_s_barrier();
  }
  __syncthreads();
  STAMP(10);
  {
    const float d0 = bf2f(rD0), d1 = bf2f(rD1);
#pragma unroll
    for (int i = 0; i < 4; i++) {
      int l = tid + 512 * i;
      float y0 = bf2f(*(LDS uint16_t*)(sm + OFF_YB + l * 2));
      float y1 = bf2f(*(LDS uint16_t*)(sm + OFF_YB + 4096 + l * 2));
      float u0 = bf2f(uu[i] & 0xFFFF), u1 = bf2f(uu[i] >> 16);
      float o0 = y0 + rbf(u0 * d0), o1 = y1 + rbf(u1 * d1);
      *(uint32_t*)(out + (size_t)l * DD + c0) = pkbf(o0, o1);
    }
  }
  while (__builtin_amdgcn_s_memrealtime() - prt0 < PADRT) __builtin_amdgcn_s_sleep(1);
}

extern "C" void kernel_launch(void* const* d_in, const int* in_sizes, int n_in, void* d_out, int out_size,
                              void* d_ws, size_t ws_size, hipStream_t stream) {
  float* dbg = nullptr;
  unsigned long long* stamps = nullptr;
  ssm_s0<<<256, 512, 0, stream>>>((const uint16_t*)d_in[0], (const uint16_t*)d_in[1], (const uint16_t*)d_in[2],
                                  (const uint16_t*)d_in[3], (const uint16_t*)d_in[4], (const uint16_t*)d_in[5],
                                  (const uint16_t*)d_in[6], (uint16_t*)d_out, dbg, stamps);
}
```

```cpp
#include <hip/hip_runtime.h>
#include <stdint.h>
#include <utility>

typedef _Float16 h2 __attribute__((ext_vector_type(2)));
typedef _Float16 h8 __attribute__((ext_vector_type(8)));
typedef __bf16 bf8 __attribute__((ext_vector_type(8)));
typedef float f4 __attribute__((ext_vector_type(4)));
typedef float f16v __attribute__((ext_vector_type(16)));
typedef uint32_t u4 __attribute__((ext_vector_type(4)));
typedef uint32_t u2 __attribute__((ext_vector_type(2)));
#define LDS __attribute__((address_space(3)))

#define STAMP(i)
#define SB() __builtin_amdgcn_sched_barrier(0)
#define LGKM0() asm volatile("s_waitcnt lgkmcnt(0)" ::: "memory")
#define BAR() do { LGKM0(); __builtin_amdgcn_s_barrier(); asm volatile("" ::: "memory"); } while (0)

constexpr int LL = 2048, DD = 448, NN = 256;
constexpr int TROW = 2064;
constexpr int OFF_TAB = 0;
constexpr int OFF_UT = 53 * TROW;
constexpr int UT_CH = (256 + 2048) * 2;
constexpr int OFF_KR = OFF_UT + 2 * UT_CH;
constexpr int KR_COPY = 4160, KR_CH = 8320;
constexpr int SMEM = OFF_KR + 2 * KR_CH;
constexpr int OFF_YX = 0;
constexpr int OFF_OT = 32768;
constexpr int OFF_YB = 40960;
constexpr int OFF_CNT = SMEM;
constexpr int SMEM2 = SMEM + 64;
constexpr int AUX_POLL = 16;
constexpr uint32_t WS_YT = 0, WS_EP = 4u << 20, WS_BYTES = (4u << 20) + 4096;
#define SPIN_MAX_V (1<<16)
constexpr int SPIN_MAX = SPIN_MAX_V;

static __device__ __forceinline__ float bf2f(uint32_t x) { return __uint_as_float(x << 16); }
static __device__ __forceinline__ float bfhi(uint32_t x) { return __uint_as_float(x & 0xFFFF0000u); }
static __device__ __forceinline__ float rbf(float f) {
  uint32_t u = __float_as_uint(f);
  return __uint_as_float((u + 0x7FFFu + ((u >> 16) & 1u)) & 0xFFFF0000u);
}
static __device__ __forceinline__ uint32_t pkbf(float lo, float hi) {
  typedef __bf16 b2 __attribute__((ext_vector_type(2)));
  typedef float f2 __attribute__((ext_vector_type(2)));
  f2 v = {lo, hi};
  b2 r = __builtin_convertvector(v, b2);
  return __builtin_bit_cast(uint32_t, r);
}
static __device__ __forceinline__ h2 cmul16(h2 x, h2 w) {
  h2 t = x * w.xx;
  h2 xs = {-x.y, x.x};
  return __builtin_elementwise_fma(xs, w.yy, t);
}
struct c32 { float r, i; };
static __device__ __forceinline__ c32 csq(c32 x) {
  c32 o; float t = x.i * x.i; o.r = __builtin_fmaf(x.r, x.r, -t); float p = x.r + x.r; o.i = p * x.i; return o;
}
static __device__ __forceinline__ h2 toh(c32 x) { h2 o; asm("v_cvt_pk_f16_f32 %0, %1, %2" : "=v"(o) : "v"(x.r), "v"(x.i)); return o; }
static __device__ __forceinline__ h2 tohc(c32 x) { h2 o; asm("v_cvt_pk_f16_f32 %0, %1, -%2" : "=v"(o) : "v"(x.r), "v"(x.i)); return o; }
static __device__ __forceinline__ float exp_acc(float x) {
  const float L2E_HI = 1.44269502162933349609375f;
  const float L2E_LO = 1.925963033500011e-08f;
  float t = x * L2E_HI;
  float e = __builtin_fmaf(x, L2E_HI, -t);
  e = __builtin_fmaf(x, L2E_LO, e);
  float n = __builtin_rintf(t);
  float f = (t - n) + e;
  float r = __builtin_amdgcn_exp2f(f);
  return __builtin_amdgcn_ldexpf(r, (int)n);
}
static __device__ __forceinline__ c32 cexp_p(float ar2, float ai, float p) {
  const float I2P_HI = 0.15915493667125701904296875f;
  const float I2P_LO = 6.4206382432e-09f;
  float m = __builtin_amdgcn_exp2f(ar2 * p);
  float x = ai * p;
  float h = x * I2P_HI;
  float e = __builtin_fmaf(x, I2P_HI, -h);
  e = __builtin_fmaf(x, I2P_LO, e);
  float f = __builtin_amdgcn_fractf(h) + e;
  c32 o; o.r = m * __builtin_amdgcn_cosf(f); o.i = m * __builtin_amdgcn_sinf(f);
  return o;
}
static __device__ __forceinline__ uint32_t wave_max_u32(uint32_t v) {
  uint32_t t;
  t = __builtin_amdgcn_update_dpp(0u, v, 0x111, 0xf, 0xf, false); v = v > t ? v : t;
  t = __builtin_amdgcn_update_dpp(0u, v, 0x112, 0xf, 0xf, false); v = v > t ? v : t;
  t = __builtin_amdgcn_update_dpp(0u, v, 0x114, 0xf, 0xf, false); v = v > t ? v : t;
  t = __builtin_amdgcn_update_dpp(0u, v, 0x118, 0xf, 0xf, false); v = v > t ? v : t;
  t = __builtin_amdgcn_update_dpp(0u, v, 0x142, 0xa, 0xf, false); v = v > t ? v : t;
  t = __builtin_amdgcn_update_dpp(0u, v, 0x143, 0xc, 0xf, false); v = v > t ? v : t;
  return __builtin_amdgcn_readlane(v, 63);
}
template <int GB, int R0>
static __device__ __forceinline__ void tab_write8(uint32_t wb, h2 a0, h2 a1, h2 a2, h2 a3, h2 a4, h2 a5, h2 a6, h2 a7) {
  asm volatile(
      "s_add_u32 m0, %0, %9\n\ts_nop 0\n\t"
      "ds_write_addtid_b32 %1 offset:%10\n\tds_write_addtid_b32 %2 offset:%11\n\t"
      "ds_write_addtid_b32 %3 offset:%12\n\tds_write_addtid_b32 %4 offset:%13\n\t"
      "ds_write_addtid_b32 %5 offset:%14\n\tds_write_addtid_b32 %6 offset:%15\n\t"
      "ds_write_addtid_b32 %7 offset:%16\n\tds_write_addtid_b32 %8 offset:%17\n\t"
      :: "s"(wb), "v"(a0), "v"(a1), "v"(a2), "v"(a3), "v"(a4), "v"(a5), "v"(a6), "v"(a7),
         "i"(GB), "i"((R0 + 0) * TROW - GB), "i"((R0 + 1) * TROW - GB), "i"((R0 + 2) * TROW - GB), "i"((R0 + 3) * TROW - GB),
         "i"((R0 + 4) * TROW - GB), "i"((R0 + 5) * TROW - GB), "i"((R0 + 6) * TROW - GB), "i"((R0 + 7) * TROW - GB)
      : "memory", "scc");
}
template <int GB, int R0>
static __device__ __forceinline__ void tab_write5(uint32_t wb, h2 a0, h2 a1, h2 a2, h2 a3, h2 a4) {
  asm volatile(
      "s_add_u32 m0, %0, %6\n\ts_nop 0\n\t"
      "ds_write_addtid_b32 %1 offset:%7\n\tds_write_addtid_b32 %2 offset:%8\n\t"
      "ds_write_addtid_b32 %3 offset:%9\n\tds_write_addtid_b32 %4 offset:%10\n\t"
      "ds_write_addtid_b32 %5 offset:%11\n\t"
      :: "s"(wb), "v"(a0), "v"(a1), "v"(a2), "v"(a3), "v"(a4),
         "i"(GB), "i"((R0 + 0) * TROW - GB), "i"((R0 + 1) * TROW - GB), "i"((R0 + 2) * TROW - GB), "i"((R0 + 3) * TROW - GB),
         "i"((R0 + 4) * TROW - GB)
      : "memory", "scc");
}
#define PERM_LO(a, b) __builtin_amdgcn_perm((b), (a), 0x05040100u)
#define PERM_HI(a, b) __builtin_amdgcn_perm((b), (a), 0x07060302u)

static __device__ __forceinline__ void lds_signal(LDS uint32_t* p, int lane) {
  asm volatile("" ::: "memory");
  if (lane == 0) __hip_atomic_fetch_add(p, 1u, __ATOMIC_RELAXED, __HIP_MEMORY_SCOPE_WORKGROUP);
  asm volatile("" ::: "memory");
}
static __device__ __forceinline__ void lds_wait(LDS uint32_t* p, uint32_t n) {
  int spins = 0;
  while (__builtin_amdgcn_readfirstlane(__hip_atomic_load(p, __ATOMIC_RELAXED, __HIP_MEMORY_SCOPE_WORKGROUP)) < n) {
    if (++spins > (1 << 22)) break;
  }
  asm volatile("" ::: "memory");
}
static constexpr int step_phase(int i) { return i < 8 ? 0 : (i < 24 ? 1 : (i < 48 ? 2 : 3)); }
static constexpr int step_nu(int i) { return i < 8 ? i : (i < 24 ? i - 8 : (i < 48 ? i - 24 : i - 48)); }

static __device__ __forceinline__ void mfma_a(f4& acc, const u4& a, const bf8& b) {
  asm volatile("v_mfma_f32_16x16x32_bf16 %0, %1, %2, %0" : "+v"(acc) : "v"(a), "v"(b));
}
static __device__ __forceinline__ void mfma_z(f4& acc, const u4& a, const bf8& b) {
  asm volatile("v_mfma_f32_16x16x32_bf16 %0, %1, %2, 0" : "=&v"(acc) : "v"(a), "v"(b));
}
struct ConvState {
  u4 A[32];
  bf8 B[4];
  f4 acc[2][2];
  uint32_t ka[4], ub, yx;
  LDS uint32_t* cnt;
  int cch, lane;
};
template <int MU, int HALF>
static __device__ __forceinline__ void conv_ldA(ConvState& cs) {
  LDS uint32_t* pa = (LDS uint32_t*)(uintptr_t)(cs.ka[(31 - MU) >> 3]) + ((31 - MU) & 7) * 32 + 2 * HALF;
  cs.A[MU][2 * HALF] = pa[0];
  cs.A[MU][2 * HALF + 1] = pa[1];
}
template <int I>
static __device__ __forceinline__ void conv_ldB(ConvState& cs) {
  cs.B[I & 3] = *(LDS bf8*)((LDS char*)(uintptr_t)cs.ub + step_nu(I) * 128);
}
template <int S, int IDX>
static __device__ __forceinline__ void conv_preA(ConvState& cs) {
  if constexpr (IDX < 16) {
    constexpr int f = IDX >> 1;
    constexpr int mu = 8 * (S + 1) + ((f & 1) ? 7 : 3) - (f >> 1);
    conv_ldA<mu, IDX & 1>(cs);
  }
}
template <int I>
static __device__ __forceinline__ void conv_step(ConvState& cs) {
  constexpr int s = step_phase(I), nu = step_nu(I), p = s & 1;
  if constexpr (nu == 0) mfma_z(cs.acc[p][1], cs.A[8 * s + 7 - nu], cs.B[I & 3]);
  else mfma_a(cs.acc[p][1], cs.A[8 * s + 7 - nu], cs.B[I & 3]);
  SB();
  if constexpr (I + 3 < 80) {
    if constexpr (step_phase(I + 3) == 2 && step_nu(I + 3) == 16) lds_wait(cs.cnt + 8, 4);
    if constexpr (step_phase(I + 3) == 3 && step_nu(I + 3) == 24) lds_wait(cs.cnt + 9, 4);
    conv_ldB<I + 3>(cs);
  }
  SB();
  if constexpr (8 * s + 3 - nu >= 0) {
    if constexpr (nu == 0) mfma_z(cs.acc[p][0], cs.A[8 * s + 3 - nu], cs.B[I & 3]);
    else mfma_a(cs.acc[p][0], cs.A[8 * s + 3 - nu], cs.B[I & 3]);
    SB();
  }
  if constexpr (s < 3) {
    if constexpr (s == 0) { conv_preA<s, 2 * nu>(cs); conv_preA<s, 2 * nu + 1>(cs); }
    else conv_preA<s, nu>(cs);
  }
  if constexpr (s > 0 && nu == 2) {
    *(LDS f4*)((LDS char*)(uintptr_t)cs.yx + (s - 1) * 8192) = cs.acc[p ^ 1][0];
    *(LDS f4*)((LDS char*)(uintptr_t)cs.yx + (s - 1) * 8192 + 1024) = cs.acc[p ^ 1][1];
    lds_signal(cs.cnt + (s - 1) * 2 + cs.cch, cs.lane);
  }
  SB();
}
template <int... Is>
static __device__ __forceinline__ void conv_all(ConvState& cs, std::integer_sequence<int, Is...>) {
  (conv_step<Is>(cs), ...);
}

__global__ __launch_bounds__(512, 2) void ssm_s5(const uint16_t* __restrict__ u, const uint16_t* __restrict__ logA,
                                                 const uint16_t* __restrict__ Aim, const uint16_t* __restrict__ Cre,
                                                 const uint16_t* __restrict__ Cim, const uint16_t* __restrict__ logdt,
                                                 const uint16_t* __restrict__ Dsk, uint16_t* __restrict__ out,
                                                 char* ws) {
  __shared__ __attribute__((aligned(16))) char smem[SMEM2];
  LDS char* sm = (LDS char*)smem;
  LDS uint32_t* cnt = (LDS uint32_t*)(sm + OFF_CNT);
  const int b = blockIdx.x;
  const int slot = b & 7, k = b >> 3;
  const int x = (slot < 4) ? slot : (11 - slot);
  if (x == 7) return;
  const int c0 = 64 * x + 2 * k;
  const int tid = threadIdx.x, lane = tid & 63;
  const int w = __builtin_amdgcn_readfirstlane(tid >> 6);
  const int ch = w >> 2;
  const int c = c0 + ch;
  const int n = (w & 3) * 64 + lane;
  STAMP(0);
  uint32_t rA = logA[c * NN + n], rI = Aim[c * NN + n], rCr = Cre[c * NN + n], rCi = Cim[c * NN + n];
  uint32_t rdt = logdt[c];
  SB();
  const int ht_ = tid & 255;
  const int piece = tid & 7;
  const uint32_t uoffb = (uint32_t)(((64 * k + (ht_ >> 3) + 32 * ch) * DD + 64 * x + 8 * piece) * 2);
  u4 uu1; uint32_t touch;
  asm volatile("global_load_dwordx4 %0, %2, %3\n\tglobal_load_dword %1, %2, %4"
               : "=&v"(uu1), "=&v"(touch) : "v"(uoffb), "s"(u), "s"(out));
  SB();
  if (tid < 64) *(LDS u4*)(sm + OFF_UT + (tid >> 5) * UT_CH + (tid & 31) * 16) = (u4){0, 0, 0, 0};
  if (tid < 16) cnt[tid] = 0;
  STAMP(1);
  asm volatile("" :: "v"(rA), "v"(rI), "v"(rCr), "v"(rCi), "v"(rdt));
  SB();
  const uint32_t nvo = (uint32_t)((tid * DD + c0) * 2);
  uint32_t ub0, ub1;
  asm volatile("global_load_dword %0, %1, %2" : "=v"(ub0) : "v"(nvo), "s"(u));
  const __amdgpu_buffer_rsrc_t wsr = __builtin_amdgcn_make_buffer_rsrc(ws, 0, WS_BYTES, 0x00020000);
  SB();
  STAMP(2);
  const float dt = rbf(exp_acc(bf2f(rdt)));
  const float ar = -rbf(exp_acc(bf2f(rA))) * dt;
  const float ai = bf2f(rI) * dt;
  const float ar2 = ar * 1.44269504088896340736f;
  c32 z1 = cexp_p(ar2, ai, 1.0f);
  c32 z128 = cexp_p(ar2, ai, 128.0f);
  c32 z2 = csq(z1), z4 = csq(z2), z8 = csq(z4), z16 = csq(z8), z32 = csq(z16), z64 = csq(z32);
  h2 e1 = tohc(z8), e2 = tohc(z16), e4 = tohc(z32), e8 = tohc(z64), e16 = tohc(z128);
  c32 z256 = csq(z128), z512 = csq(z256), z1024 = csq(z512);
  uint32_t mx = max(rCr & 0x7FFFu, rCi & 0x7FFFu);
  uint32_t eb = wave_max_u32(mx) >> 7;
  eb = eb < 1 ? 127 : (eb > 253 ? 253 : eb);
  const float sc = __uint_as_float((254u - eb) << 23);
  const float isc = __uint_as_float(eb << 23);
  c32 C; C.r = bf2f(rCr) * sc; C.i = bf2f(rCi) * sc;
  const uint32_t wb = (uint32_t)w * 256u;
  uint32_t ub2, ub3, ub4, ub5, ep, dn2;
  u4 uu0, dsk;
  const uint32_t hvo = (uint32_t)(((1024 + ht_) * DD + c0) * 2);
  {
    h2 e0 = {(_Float16)1.0f, (_Float16)0.0f};
    h2 e3 = cmul16(e1, e2), e5 = cmul16(e1, e4), e6 = cmul16(e2, e4), e7 = cmul16(e3, e4);
    tab_write8<0, 0>(wb, e0, e1, e2, e3, e4, e5, e6, e7);
    h2 e9 = cmul16(e1, e8), e10 = cmul16(e2, e8), e11 = cmul16(e3, e8), e12 = cmul16(e4, e8), e13 = cmul16(e5, e8),
       e14 = cmul16(e6, e8), e15 = cmul16(e7, e8);
    tab_write8<0, 8>(wb, e8, e9, e10, e11, e12, e13, e14, e15);
    SB();
    asm volatile("global_load_dword %0, %1, %2" : "=v"(ub1) : "v"(nvo), "s"(u + (size_t)512 * DD));
    asm volatile("s_cmp_lt_u32 %9, 4\n\ts_cbranch_scc1 1f\n\t"
                 "global_load_dwordx4 %0, %4, %5\n\t"
                 "global_load_dwordx4 %1, %6, %7\n\t"
                 "global_load_dword %2, %8, %7\n\t"
                 "global_load_dword %3, %10, %11 sc1\n\t"
                 "1:"
                 : "=&v"(uu0), "=&v"(dsk), "=&v"(dn2), "=&v"(ep)
                 : "v"(uoffb - 32 * DD * 2), "s"(u), "v"((uint32_t)((64 * x + 8 * piece) * 2)), "s"(Dsk), "v"((uint32_t)(c0 * 2)),
                   "s"(w), "v"((uint32_t)(WS_EP + x * 256)), "s"(ws)
                 : "scc");
    SB();
    tab_write8<0, 16>(wb, e16, cmul16(e1, e16), cmul16(e2, e16), cmul16(e3, e16), cmul16(e4, e16), cmul16(e5, e16),
                      cmul16(e6, e16), cmul16(e7, e16));
    tab_write8<0, 24>(wb, cmul16(e8, e16), cmul16(e9, e16), cmul16(e10, e16), cmul16(e11, e16), cmul16(e12, e16),
                      cmul16(e13, e16), cmul16(e14, e16), cmul16(e15, e16));
    h2 y1 = toh(z1), y2 = toh(z2), y4 = toh(z4), y256 = toh(z256), y512 = toh(z512), y1024 = toh(z1024);
    h2 g0 = toh(C), g1 = cmul16(g0, y1), g2 = cmul16(g0, y2), g3 = cmul16(g1, y2), g4 = cmul16(g0, y4),
       g5 = cmul16(g1, y4), g6 = cmul16(g2, y4), g7 = cmul16(g3, y4);
    tab_write8<49152, 32>(wb, g0, g1, g2, g3, g4, g5, g6, g7);
    h2 a0 = cmul16(g0, y256), a2 = cmul16(g2, y256), a4 = cmul16(g4, y256), a6 = cmul16(g6, y256);
    h2 b0 = cmul16(g0, y512), b4 = cmul16(g4, y512), d0 = cmul16(a0, y512), d4 = cmul16(a4, y512);
    tab_write8<49152, 40>(wb, a0, a2, a4, a6, b0, b4, d0, d4);
    h2 f0 = cmul16(g0, y1024), f1 = cmul16(a0, y1024), f2_ = cmul16(b0, y1024), f3 = cmul16(d0, y1024),
       f4_ = cmul16(f0, y1024);
    tab_write5<49152, 48>(wb, f0, f1, f2_, f3, f4_);
  }
  STAMP(3);
  uint32_t aM, aX, aX2;
  const int ip = 255 - ht_;
  const int er = ip & 31;
  const int wq = w & 3;
  {
    const int r = lane & 31, h = lane >> 5;
    LDS char* pb = sm + OFF_TAB + r * TROW + w * 256 + h * 16;
    LDS char* pa = pb + 32 * TROW;
    h8 av[8], bv[8];
#pragma unroll
    for (int s = 0; s < 8; s++) { av[s] = *(LDS h8*)(pa + s * 32); bv[s] = *(LDS h8*)(pb + s * 32); }
    SB();
    asm volatile("s_cmp_lt_u32 %5, 4\n\ts_cbranch_scc1 1f\n\t"
                 "global_load_dword %0, %2, %3\n\tglobal_load_dword %1, %2, %4\n\t1:"
                 : "=&v"(ub2), "=&v"(ub3) : "v"(hvo), "s"(u), "s"(u + (size_t)256 * DD), "s"(w) : "scc");
    {
      const uint32_t pw = (uint32_t)(uintptr_t)(sm + OFF_TAB) + ch * 1024;
      const uint32_t pr = pw + er * TROW;
      const int beta3 = (ip - 128) >> 5, beta2 = (ip - 64) >> 5;
      const bool r1 = ip >= 32;
      int cM, cN, cW;
      if (wq < 2) { cM = 16; cN = 16 + beta3; cW = 17 + beta3; }
      else if (wq == 2) { cM = 12; cN = 12 + 2 * beta2; cW = 14 + 2 * beta2; }
      else { cM = r1 ? 8 : 0; cN = 8; cW = 12; }
      aM = pr + cM * 4;
      aX = (er == 31) ? pw + cW * 4 : pr + TROW + cN * 4;
      aX2 = pw + ((er == 0) ? 31 : er - 1) * TROW + 7 * 4;
      asm volatile("" : "+v"(aM), "+v"(aX), "+v"(aX2));
    }
    SB();
    f16v acc = {0};
#pragma unroll
    for (int s = 0; s < 8; s++) acc = __builtin_amdgcn_mfma_f32_32x32x16_f16(av[s], bv[s], acc, 0, 0, 0);
    const float ks = 2.0f * dt * isc;
#pragma unroll
    for (int g = 0; g < 3; g++) {
      f4 v = {acc[4 * g] * ks, acc[4 * g + 1] * ks, acc[4 * g + 2] * ks, acc[4 * g + 3] * ks};
      *(LDS f4*)(pb + g * 32) = v;
    }
  }
  STAMP(4);
  asm volatile("s_cmp_lt_u32 %4, 4\n\ts_cbranch_scc1 1f\n\ts_waitcnt vmcnt(6)\n\ts_branch 2f\n\t1:\n\ts_waitcnt vmcnt(0)\n\t2:"
               : "+v"(ub0), "+v"(ub1), "+v"(uu1), "+v"(touch) : "s"(w) : "scc", "memory");
  {
    LDS uint16_t* ut = (LDS uint16_t*)(sm + OFF_UT) + 256 + tid;
    ut[0] = (uint16_t)ub0; ut[UT_CH / 2] = (uint16_t)(ub0 >> 16);
    ut[512] = (uint16_t)ub1; ut[UT_CH / 2 + 512] = (uint16_t)(ub1 >> 16);
  }
  STAMP(5);
  BAR();
  STAMP(6);
  asm volatile("s_cmp_lt_u32 %5, 4\n\ts_cbranch_scc1 1f\n\t"
               "global_load_dword %0, %2, %3\n\tglobal_load_dword %1, %2, %4\n\t1:"
               : "=&v"(ub4), "=&v"(ub5) : "v"(hvo), "s"(u + (size_t)512 * DD), "s"(u + (size_t)768 * DD), "s"(w) : "scc");
  {
    LDS char* pM = (LDS char*)(uintptr_t)aM;
    LDS char* pX = (LDS char*)(uintptr_t)aX;
    f4 m0 = *(LDS f4*)pM, m1 = *(LDS f4*)(pM + 256), m2 = *(LDS f4*)(pM + 512), m3 = *(LDS f4*)(pM + 768);
    float x0 = *(LDS float*)pX, x1 = *(LDS float*)(pX + 256), x2 = *(LDS float*)(pX + 512), x3 = *(LDS float*)(pX + 768);
    float val[8], prev;
    if (wq == 3) {
      LDS char* pN = (LDS char*)(uintptr_t)(aM & ~0x3Fu) + 16;
      LDS char* pP = (LDS char*)(uintptr_t)aX2;
      f4 n0 = *(LDS f4*)(pM + 16), n1 = *(LDS f4*)(pM + 16 + 256), n2 = *(LDS f4*)(pM + 16 + 512), n3 = *(LDS f4*)(pM + 16 + 768);
      float p0 = *(LDS float*)pP, p1 = *(LDS float*)(pP + 256), p2 = *(LDS float*)(pP + 512), p3 = *(LDS float*)(pP + 768);
      (void)pN;
      SB();
      f4 q0 = (m0 + m1) + (m2 + m3), q1 = (n0 + n1) + (n2 + n3);
      float nx = (x0 + x1) + (x2 + x3), pv = (p0 + p1) + (p2 + p3);
      const bool r1 = ip >= 32;
      val[0] = q0[0];
      val[1] = r1 ? q0[0] : q0[1];
      val[2] = r1 ? q0[1] : q0[2];
      val[3] = r1 ? q0[2] : q0[3];
      val[4] = r1 ? q0[2] : q1[0];
      val[5] = r1 ? q0[2] : q1[1];
      val[6] = r1 ? q0[3] : q1[2];
      val[7] = r1 ? nx : q1[3];
      prev = r1 ? (er == 0 ? pv : q0[0]) : (er == 0 ? 0.0f : pv);
    } else {
      SB();
      f4 q = (m0 + m1) + (m2 + m3);
      float nx = (x0 + x1) + (x2 + x3);
      if (wq == 2) {
        const bool be = ip >= 96;
        float b0 = be ? q[2] : q[0], b1 = be ? q[3] : q[1];
        val[0] = val[1] = val[2] = b0; val[3] = val[4] = val[5] = b1; val[6] = val[7] = nx; prev = b0;
      } else {
        const int beta = (ip - 128) >> 5;
        float lo2 = (beta & 1) ? q[1] : q[0], hi2 = (beta & 1) ? q[3] : q[2];
        float d0 = (beta & 2) ? hi2 : lo2;
        float mid = (ip & 1) ? nx : d0;
        val[0] = val[1] = val[2] = val[3] = d0; val[4] = mid; val[5] = val[6] = val[7] = nx; prev = d0;
      }
    }
    u4 c0v = {pkbf(val[7], val[6]), pkbf(val[5], val[4]), pkbf(val[3], val[2]), pkbf(val[1], val[0])};
    u4 c1v = {pkbf(val[6], val[5]), pkbf(val[4], val[3]), pkbf(val[2], val[1]), pkbf(val[0], prev)};
    LDS char* kr = sm + OFF_KR + ch * KR_CH;
    *(LDS u4*)(kr + ht_ * 16) = c0v;
    *(LDS u4*)(kr + KR_COPY + ht_ * 16) = c1v;
    if (ht_ < 8) *(LDS u4*)(kr + (ht_ >> 2) * KR_COPY + 4096 + (ht_ & 3) * 16) = (u4){0, 0, 0, 0};
  }
  STAMP(7);
  BAR();
  STAMP(8);
  if (w < 4) {
    const int cch = w >> 1, par = w & 1;
    const int bb = lane & 15, q = lane >> 4;
    uint32_t ub = (uint32_t)(uintptr_t)(sm + OFF_UT + cch * UT_CH + (256 + 32 * (-6 - par) - 16 + 16 * bb + 8 * q) * 2);
    int x0 = 2031 - 32 * par - bb + 8 * q;
    uint32_t ka0 = (uint32_t)(uintptr_t)(sm + OFF_KR + cch * KR_CH + ((bb & 1) ? 2 * x0 : (KR_COPY + 2 * (x0 - 1))) - 31 * 128);
    uint32_t ka[4] = {ka0, ka0 + 1024, ka0 + 2048, ka0 + 3072};
    uint32_t yx = (uint32_t)(uintptr_t)(sm + OFF_YX + (cch * 2 + par) * 2048 + (16 * q + ((bb + 4 * q) & 15)) * 16);
    asm volatile("" : "+v"(ka[0]), "+v"(ka[1]), "+v"(ka[2]), "+v"(ka[3]), "+v"(ub), "+v"(yx));
    ConvState cs;
    cs.ka[0] = ka[0]; cs.ka[1] = ka[1]; cs.ka[2] = ka[2]; cs.ka[3] = ka[3]; cs.ub = ub; cs.yx = yx;
    cs.cnt = cnt; cs.cch = cch; cs.lane = lane;
    conv_ldA<3, 0>(cs); conv_ldA<3, 1>(cs); conv_ldA<7, 0>(cs); conv_ldA<7, 1>(cs); conv_ldB<0>(cs);
    conv_ldA<2, 0>(cs); conv_ldA<2, 1>(cs); conv_ldA<6, 0>(cs); conv_ldA<6, 1>(cs); conv_ldB<1>(cs);
    conv_ldA<1, 0>(cs); conv_ldA<1, 1>(cs); conv_ldA<5, 0>(cs); conv_ldA<5, 1>(cs); conv_ldB<2>(cs);
    conv_ldA<0, 0>(cs); conv_ldA<0, 1>(cs); conv_ldA<4, 0>(cs); conv_ldA<4, 1>(cs);
    SB();
    conv_all(cs, std::make_integer_sequence<int, 80>{});
    f4 (&acc)[2][2] = cs.acc;
    asm volatile("s_nop 7\n\ts_nop 3" ::: "memory");
    *(LDS f4*)((LDS char*)(uintptr_t)yx + 3 * 8192) = acc[1][0];
    *(LDS f4*)((LDS char*)(uintptr_t)yx + 3 * 8192 + 1024) = acc[1][1];
    lds_signal(cnt + 3 * 2 + cch, lane);
    STAMP(9);
  } else {
    const int h = w - 4, hc = h >> 1, ht = h & 1;
    asm volatile("s_waitcnt vmcnt(2)" : "+v"(ub2), "+v"(ub3), "+v"(uu0), "+v"(dsk), "+v"(dn2), "+v"(ep) :: "memory");
    const uint32_t tag = (((uint32_t)__builtin_amdgcn_readfirstlane(ep) + 1u) << 1) | 1u;
    LDS uint16_t* ut = (LDS uint16_t*)(sm + OFF_UT) + 256 + 1024 + ht_;
    ut[0] = (uint16_t)ub2; ut[UT_CH / 2] = (uint16_t)(ub2 >> 16);
    ut[256] = (uint16_t)ub3; ut[UT_CH / 2 + 256] = (uint16_t)(ub3 >> 16);
    lds_signal(cnt + 8, lane);
    STAMP(14);
    const int a = lane >> 2, q = lane & 3;
    LDS char* yx = sm + OFF_YX + hc * 4096 + ht * 1024 + (16 * q + ((a + 4 * q) & 15)) * 16;
    const uint32_t yo = WS_YT + ((c0 + hc) * 512 + ht * 64 + lane) * 16;
    {
      lds_wait(cnt + hc, 2);
      f4 p0 = *(LDS f4*)(yx), p1 = *(LDS f4*)(yx + 2048);
      f4 v = p0 + p1;
      __builtin_amdgcn_raw_buffer_store_b128((u4){pkbf(v[0], v[1]), tag, pkbf(v[2], v[3]), tag}, wsr, yo, 0, 16);
    }
    asm volatile("s_waitcnt vmcnt(1)" : "+v"(ub4), "+v"(ub5) :: "memory");
    ut[512] = (uint16_t)ub4; ut[UT_CH / 2 + 512] = (uint16_t)(ub4 >> 16);
    ut[768] = (uint16_t)ub5; ut[UT_CH / 2 + 768] = (uint16_t)(ub5 >> 16);
    lds_signal(cnt + 9, lane);
    STAMP(9);
#pragma unroll
    for (int s = 1; s < 3; s++) {
      lds_wait(cnt + s * 2 + hc, 2);
      f4 p0 = *(LDS f4*)(yx + s * 8192), p1 = *(LDS f4*)(yx + s * 8192 + 2048);
      f4 v = p0 + p1;
      __builtin_amdgcn_raw_buffer_store_b128((u4){pkbf(v[0], v[1]), tag, pkbf(v[2], v[3]), tag}, wsr, yo + s * 128 * 16, 0, 16);
    }
    STAMP(10);
    const int pp = ht_ >> 4, ps = ht_ & 15;
    const uint32_t go = WS_YT + ((64 * x + 2 * pp) * 512 + 16 * k + ps) * 16;
    u4 g0, g1, g2, g3;
    {
      const int rr = 128 * h + 2 * lane;
      const int tl = rr >> 8, pos = rr & 255;
      const int aa = pos >> 4, qq = (pos >> 2) & 3, ii = pos & 3;
      LDS char* yp = sm + OFF_YX + 3 * 8192 + tl * 1024 + (16 * qq + ((aa + 4 * qq) & 15)) * 16 + ii * 4;
      uint32_t ua = *(LDS uint32_t*)(sm + OFF_UT + (256 + 1536 + rr) * 2);
      uint32_t ubb = *(LDS uint32_t*)(sm + OFF_UT + UT_CH + (256 + 1536 + rr) * 2);
      const float d0 = bf2f(dn2 & 0xFFFF), d1 = bfhi(dn2);
      float s00 = rbf(bf2f(ua & 0xFFFF) * d0), s01 = rbf(bfhi(ua) * d0);
      float s10 = rbf(bf2f(ubb & 0xFFFF) * d1), s11 = rbf(bfhi(ubb) * d1);
      lds_wait(cnt + 6, 2);
      lds_wait(cnt + 7, 2);
      typedef float f2v __attribute__((ext_vector_type(2)));
      f2v y00 = *(LDS f2v*)(yp), y01 = *(LDS f2v*)(yp + 2048), y10 = *(LDS f2v*)(yp + 4096), y11 = *(LDS f2v*)(yp + 6144);
      f2v ya = y00 + y01, yb = y10 + y11;
      uint32_t pa = pkbf(ya[0], ya[1]), pb = pkbf(yb[0], yb[1]);
      float o00 = bf2f(pa & 0xFFFF) + s00, o01 = bfhi(pa) + s01, o10 = bf2f(pb & 0xFFFF) + s10, o11 = bfhi(pb) + s11;
      uint16_t* op = out + (size_t)(1536 + rr) * DD + c0;
      g0 = __builtin_amdgcn_raw_buffer_load_b128(wsr, go, 0, 16);
      g1 = __builtin_amdgcn_raw_buffer_load_b128(wsr, go + 512 * 16, 0, 16);
      g2 = __builtin_amdgcn_raw_buffer_load_b128(wsr, go + 32 * 512 * 16, 0, 16);
      g3 = __builtin_amdgcn_raw_buffer_load_b128(wsr, go + 33 * 512 * 16, 0, 16);
      SB();
      *(uint32_t*)(op) = pkbf(o00, o10);
      *(uint32_t*)(op + DD) = pkbf(o01, o11);
    }
    STAMP(11);
    if (k < 24) {
      int spins = 0;
      while (true) {
        bool ok = (g0[1] == tag) & (g0[3] == tag) & (g1[1] == tag) & (g1[3] == tag) &
                  (g2[1] == tag) & (g2[3] == tag) & (g3[1] == tag) & (g3[3] == tag);
        if (__all(ok) || ++spins > SPIN_MAX) break;
        asm volatile("" ::: "memory");
        g0 = __builtin_amdgcn_raw_buffer_load_b128(wsr, go, 0, 16);
        g1 = __builtin_amdgcn_raw_buffer_load_b128(wsr, go + 512 * 16, 0, 16);
        g2 = __builtin_amdgcn_raw_buffer_load_b128(wsr, go + 32 * 512 * 16, 0, 16);
        g3 = __builtin_amdgcn_raw_buffer_load_b128(wsr, go + 33 * 512 * 16, 0, 16);
      }
      STAMP(12);
      LDS uint32_t* ot = (LDS uint32_t*)(sm + OFF_OT) + (4 * ps) * 32 + (((pp >> 2) ^ (ps & 7)) << 2) + (pp & 3);
      ot[0] = PERM_LO(g0[0], g1[0]); ot[32] = PERM_HI(g0[0], g1[0]);
      ot[64] = PERM_LO(g0[2], g1[2]); ot[96] = PERM_HI(g0[2], g1[2]);
      LDS uint32_t* ot2 = (LDS uint32_t*)(sm + OFF_OT) + (4 * ps) * 32 + ((((pp >> 2) + 4) ^ (ps & 7)) << 2) + (pp & 3);
      ot2[0] = PERM_LO(g2[0], g3[0]); ot2[32] = PERM_HI(g2[0], g3[0]);
      ot2[64] = PERM_LO(g2[2], g3[2]); ot2[96] = PERM_HI(g2[2], g3[2]);
      lds_signal(cnt + 11, lane);
      lds_wait(cnt + 11, 4);
#pragma unroll
      for (int e = 0; e < 2; e++) {
        const int row = (ht_ >> 3) + 32 * e;
        u4 y = *(LDS u4*)(sm + OFF_OT + row * 128 + ((piece ^ ((row >> 2) & 7)) * 16));
        const u4 uu = e ? uu1 : uu0;
        u4 o;
#pragma unroll
        for (int j = 0; j < 4; j++) {
          float lo = bf2f(y[j] & 0xFFFF) + rbf(bf2f(uu[j] & 0xFFFF) * bf2f(dsk[j] & 0xFFFF));
          float hi = bfhi(y[j]) + rbf(bfhi(uu[j]) * bfhi(dsk[j]));
          o[j] = pkbf(lo, hi);
        }
        *(u4*)((char*)out + uoffb - 32 * DD * 2 * (1 - e)) = o;
      }
      if (k == 0 && ht_ == 0) __builtin_amdgcn_raw_buffer_store_b32(tag >> 1, wsr, WS_EP + x * 256, 0, 16);
    }
    STAMP(13);
  }
}

extern "C" void kernel_launch(void* const* d_in, const int* in_sizes, int n_in, void* d_out, int out_size,
                              void* d_ws, size_t ws_size, hipStream_t stream) {
  ssm_s5<<<256, 512, 0, stream>>>((const uint16_t*)d_in[0], (const uint16_t*)d_in[1], (const uint16_t*)d_in[2],
                                  (const uint16_t*)d_in[3], (const uint16_t*)d_in[4], (const uint16_t*)d_in[5],
                                  (const uint16_t*)d_in[6], (uint16_t*)d_out, (char*)d_ws);
}
```

```cpp
#include <hip/hip_runtime.h>
#include <stdint.h>

typedef _Float16 h2 __attribute__((ext_vector_type(2)));
typedef _Float16 h8 __attribute__((ext_vector_type(8)));
typedef __bf16 bf8 __attribute__((ext_vector_type(8)));
typedef float f2v __attribute__((ext_vector_type(2)));
typedef float f4 __attribute__((ext_vector_type(4)));
typedef float f16v __attribute__((ext_vector_type(16)));
typedef uint32_t u4 __attribute__((ext_vector_type(4)));
typedef uint32_t u2 __attribute__((ext_vector_type(2)));
#define LDS __attribute__((address_space(3)))

#define SB() __builtin_amdgcn_sched_barrier(0)
#define BAR() do { asm volatile("s_waitcnt lgkmcnt(0)" ::: "memory"); __builtin_amdgcn_s_barrier(); asm volatile("" ::: "memory"); } while (0)

constexpr int LL = 2048, DD = 448, NN = 256;
constexpr int TROW = 2064;
constexpr int OFF_TAB = 0;
constexpr int OFF_UT = 53 * TROW;
constexpr int UT_CH = (256 + 2048) * 2;
constexpr int OFF_KR = OFF_UT + 2 * UT_CH;
constexpr int KR_COPY = 4160, KR_CH = 8320;
constexpr int OFF_YX = 0;
constexpr int OFF_CNT = OFF_KR + 2 * KR_CH;
constexpr int SMEM = OFF_CNT + 256;
constexpr int FL_U = 32, FL_TAG = 36;
constexpr uint32_t WS_UT = 0;
constexpr uint32_t WS_YT = 2u << 20;
constexpr uint32_t WS_ACK = 4u << 20;
constexpr uint32_t WS_EP = (4u << 20) + 8192;
constexpr uint32_t WS_BYTES = (4u << 20) + 16384;
constexpr int SC1 = 16;
constexpr int SPIN_MAX = 1 << 16;
constexpr unsigned TGATE = 350;
constexpr int PSL = 4;

static __device__ __forceinline__ float bf2f(uint32_t x) { return __uint_as_float(x << 16); }
static __device__ __forceinline__ float bflo(uint32_t x) { return __uint_as_float(x << 16); }
static __device__ __forceinline__ float bfhi(uint32_t x) { return __uint_as_float(x & 0xFFFF0000u); }
static __device__ __forceinline__ uint32_t pkbf(float lo, float hi) {
  typedef __bf16 b2 __attribute__((ext_vector_type(2)));
  f2v v = {lo, hi};
  b2 r = __builtin_convertvector(v, b2);
  return __builtin_bit_cast(uint32_t, r);
}
static __device__ __forceinline__ h2 cmul16(h2 x, h2 w) {
  h2 t = x * w.xx;
  h2 xs = {-x.y, x.x};
  return __builtin_elementwise_fma(xs, w.yy, t);
}
struct c32 { float r, i; };
struct c32x2 { f2v r, i; };
static __device__ __forceinline__ c32x2 csq2(c32x2 x) {
  c32x2 o; f2v t = x.i * x.i; o.r = __builtin_elementwise_fma(x.r, x.r, -t); f2v p = x.r + x.r; o.i = p * x.i; return o;
}
static __device__ __forceinline__ c32 csq(c32 x) {
  c32 o; float t = x.i * x.i; o.r = __builtin_fmaf(x.r, x.r, -t); float p = x.r + x.r; o.i = p * x.i; return o;
}
static __device__ __forceinline__ h2 toh(c32 x) { h2 o; asm("v_cvt_pk_f16_f32 %0, %1, %2" : "=v"(o) : "v"(x.r), "v"(x.i)); return o; }
static __device__ __forceinline__ h2 tohc(c32 x) { h2 o; asm("v_cvt_pk_f16_f32 %0, %1, -%2" : "=v"(o) : "v"(x.r), "v"(x.i)); return o; }
static __device__ __forceinline__ uint32_t wave_max_u32(uint32_t v) {
  uint32_t t;
  t = __builtin_amdgcn_update_dpp(0u, v, 0x111, 0xf, 0xf, false); v = v > t ? v : t;
  t = __builtin_amdgcn_update_dpp(0u, v, 0x112, 0xf, 0xf, false); v = v > t ? v : t;
  t = __builtin_amdgcn_update_dpp(0u, v, 0x114, 0xf, 0xf, false); v = v > t ? v : t;
  t = __builtin_amdgcn_update_dpp(0u, v, 0x118, 0xf, 0xf, false); v = v > t ? v : t;
  t = __builtin_amdgcn_update_dpp(0u, v, 0x142, 0xa, 0xf, false); v = v > t ? v : t;
  t = __builtin_amdgcn_update_dpp(0u, v, 0x143, 0xc, 0xf, false); v = v > t ? v : t;
  return __builtin_amdgcn_readlane(v, 63);
}
static __device__ __forceinline__ void tab_base(uint32_t v) { asm volatile("s_mov_b32 m0, %0\n\ts_nop 0" :: "s"(v) : "memory"); }
template <int GB, int R>
static __device__ __forceinline__ void tw(h2 a) {
  asm volatile("ds_write_addtid_b32 %0 offset:%1" :: "v"(a), "i"(R * TROW - GB) : "memory");
}
#define PERM_LO(a, b) __builtin_amdgcn_perm((b), (a), 0x05040100u)
#define PERM_HI(a, b) __builtin_amdgcn_perm((b), (a), 0x07060302u)
static __device__ __forceinline__ uint32_t make_tag(uint32_t epoch) { return ((epoch + 1u) << 1) | 1u; }

static __device__ __forceinline__ void flag_set(LDS uint32_t* p, uint32_t v = 1u) {
  asm volatile("" ::: "memory");
  *(volatile LDS uint32_t*)p = v;
  asm volatile("" ::: "memory");
}
static __device__ __forceinline__ void flag_wait4(LDS uint32_t* p) {
  for (int spins = 0; spins < (1 << 22); spins++) {
    u4 v = *(volatile LDS u4*)p;
    if (__builtin_amdgcn_readfirstlane(v[0] & v[1] & v[2] & v[3]) != 0) break;
  }
  asm volatile("" ::: "memory");
}
static __device__ __forceinline__ void flag_wait2(LDS uint32_t* p) {
  for (int spins = 0; spins < (1 << 22); spins++) {
    u2 v = *(volatile LDS u2*)p;
    if (__builtin_amdgcn_readfirstlane(v[0] & v[1]) != 0) break;
  }
  asm volatile("" ::: "memory");
}

static __device__ __forceinline__ void mfma_a(f4& acc, const u4& a, const bf8& b) {
  asm volatile("v_mfma_f32_16x16x32_bf16 %0, %1, %2, %0" : "+v"(acc) : "v"(a), "v"(b));
}
static __device__ __forceinline__ void mfma_z(f4& acc, const u4& a, const bf8& b) {
  asm volatile("v_mfma_f32_16x16x32_bf16 %0, %1, %2, 0" : "=&v"(acc) : "v"(a), "v"(b));
}
struct ConvState {
  u4 A[32];
  bf8 B[4];
  f4 acc[8];
  u4 gate;
  uint32_t ka[4], ub, yx;
  LDS uint32_t* cnt;
  int wid;
};
template <int MU, int HALF>
static __device__ __forceinline__ void conv_ldA(ConvState& cs) {
  LDS uint32_t* pa = (LDS uint32_t*)(uintptr_t)(cs.ka[(31 - MU) >> 3]) + ((31 - MU) & 7) * 32 + 2 * HALF;
  cs.A[MU][2 * HALF] = pa[0];
  cs.A[MU][2 * HALF + 1] = pa[1];
}
template <int SLOT, int NU>
static __device__ __forceinline__ void conv_ldB(ConvState& cs) {
  cs.B[SLOT] = *(LDS bf8*)((LDS char*)(uintptr_t)cs.ub + NU * 128);
}
static __device__ __forceinline__ void conv_gpre(ConvState& cs) { cs.gate = *(volatile LDS u4*)(cs.cnt + FL_U); }
template <int G>
static __device__ __forceinline__ void conv_gate(ConvState& cs) {
  if (__builtin_amdgcn_readfirstlane(cs.gate[2 * G] & cs.gate[2 * G + 1]) == 0) flag_wait2(cs.cnt + FL_U + 2 * G);
}
template <int T>
static __device__ __forceinline__ void conv_hand(ConvState& cs) {
  asm volatile("s_nop 5" ::: "memory");
  *(LDS f4*)((LDS char*)(uintptr_t)cs.yx + T * 4096) = cs.acc[T];
  flag_set(cs.cnt + T * 4 + cs.wid);
}
static __device__ __forceinline__ void conv_pre(ConvState& cs) {
  conv_ldB<0,0>(cs); conv_ldB<1,1>(cs); conv_ldB<2,2>(cs);
}
static __device__ __forceinline__ void conv_run(ConvState& cs) {
  conv_ldA<15,0>(cs); conv_ldA<11,0>(cs); conv_ldA<7,0>(cs); conv_ldA<3,0>(cs); conv_ldA<15,1>(cs); conv_ldA<11,1>(cs); conv_ldA<7,1>(cs); conv_ldA<3,1>(cs); conv_ldA<2,0>(cs); conv_ldA<6,0>(cs); conv_ldA<10,0>(cs); conv_ldA<14,0>(cs); conv_ldA<6,1>(cs); conv_ldA<10,1>(cs); conv_ldA<14,1>(cs); SB();
  mfma_z(cs.acc[3], cs.A[15], cs.B[0]); SB(); conv_ldB<3,3>(cs); conv_ldA<2,1>(cs); conv_ldA<5,0>(cs); conv_ldA<13,0>(cs); SB();
  mfma_z(cs.acc[2], cs.A[11], cs.B[0]); SB(); conv_ldA<5,1>(cs); conv_ldA<13,1>(cs); conv_ldA<8,0>(cs); SB();
  mfma_z(cs.acc[1], cs.A[7], cs.B[0]); SB(); conv_ldA<1,0>(cs); conv_ldA<9,0>(cs); conv_ldA<8,1>(cs); SB();
  mfma_z(cs.acc[0], cs.A[3], cs.B[0]); SB(); conv_ldA<1,1>(cs); conv_ldA<9,1>(cs); conv_ldA<4,0>(cs); SB();
  mfma_a(cs.acc[3], cs.A[14], cs.B[1]); SB(); conv_ldB<0,4>(cs); SB();
  mfma_a(cs.acc[2], cs.A[10], cs.B[1]); SB(); conv_ldA<4,1>(cs); SB();
  mfma_a(cs.acc[1], cs.A[6], cs.B[1]); SB(); conv_ldA<0,0>(cs); conv_ldA<12,0>(cs); SB();
  mfma_a(cs.acc[0], cs.A[2], cs.B[1]); SB(); conv_ldA<0,1>(cs); conv_ldA<12,1>(cs); SB();
  mfma_a(cs.acc[3], cs.A[13], cs.B[2]); SB(); conv_ldB<1,5>(cs); SB();
  mfma_a(cs.acc[2], cs.A[9], cs.B[2]); SB(); SB();
  mfma_a(cs.acc[1], cs.A[5], cs.B[2]); SB(); SB();
  mfma_a(cs.acc[0], cs.A[1], cs.B[2]); SB(); SB();
  mfma_a(cs.acc[3], cs.A[12], cs.B[3]); SB(); conv_ldB<2,6>(cs); SB();
  mfma_a(cs.acc[2], cs.A[8], cs.B[3]); SB(); conv_ldA<23,0>(cs); SB();
  mfma_a(cs.acc[1], cs.A[4], cs.B[3]); SB(); conv_ldA<23,1>(cs); SB();
  mfma_a(cs.acc[0], cs.A[0], cs.B[3]); SB(); conv_ldA<19,0>(cs); SB();
  mfma_a(cs.acc[3], cs.A[11], cs.B[0]); SB(); conv_ldB<3,7>(cs); SB();
  mfma_a(cs.acc[2], cs.A[7], cs.B[0]); SB(); conv_ldA<19,1>(cs); conv_hand<0>(cs); SB();
  mfma_a(cs.acc[1], cs.A[3], cs.B[0]); SB(); conv_ldA<22,0>(cs); SB();
  mfma_a(cs.acc[3], cs.A[10], cs.B[1]); SB(); conv_ldB<0,8>(cs); SB();
  mfma_a(cs.acc[2], cs.A[6], cs.B[1]); SB(); conv_ldA<22,1>(cs); SB();
  mfma_a(cs.acc[1], cs.A[2], cs.B[1]); SB(); conv_ldA<18,0>(cs); SB();
  mfma_a(cs.acc[3], cs.A[9], cs.B[2]); SB(); conv_ldB<1,9>(cs); SB();
  mfma_a(cs.acc[2], cs.A[5], cs.B[2]); SB(); conv_ldA<18,1>(cs); SB();
  mfma_a(cs.acc[1], cs.A[1], cs.B[2]); SB(); conv_ldA<21,0>(cs); SB();
  mfma_a(cs.acc[3], cs.A[8], cs.B[3]); SB(); conv_ldB<2,10>(cs); SB();
  mfma_a(cs.acc[2], cs.A[4], cs.B[3]); SB(); conv_ldA<21,1>(cs); SB();
  mfma_a(cs.acc[1], cs.A[0], cs.B[3]); SB(); conv_ldA<17,0>(cs); SB();
  mfma_a(cs.acc[3], cs.A[7], cs.B[0]); SB(); conv_ldB<3,11>(cs); SB();
  mfma_a(cs.acc[2], cs.A[3], cs.B[0]); SB(); conv_ldA<17,1>(cs); conv_hand<1>(cs); SB();
  mfma_a(cs.acc[3], cs.A[6], cs.B[1]); SB(); conv_ldB<0,12>(cs); SB();
  mfma_a(cs.acc[2], cs.A[2], cs.B[1]); SB(); conv_ldA<20,0>(cs); SB();
  mfma_a(cs.acc[3], cs.A[5], cs.B[2]); SB(); conv_ldB<1,13>(cs); SB();
  mfma_a(cs.acc[2], cs.A[1], cs.B[2]); SB(); conv_ldA<20,1>(cs); SB();
  mfma_a(cs.acc[3], cs.A[4], cs.B[3]); SB(); conv_ldB<2,14>(cs); SB();
  mfma_a(cs.acc[2], cs.A[0], cs.B[3]); SB(); conv_ldA<16,0>(cs); SB();
  mfma_a(cs.acc[3], cs.A[3], cs.B[0]); SB(); conv_ldB<3,15>(cs); SB();
  mfma_a(cs.acc[3], cs.A[2], cs.B[1]); SB(); conv_ldB<0,0>(cs); conv_hand<2>(cs); SB();
  mfma_a(cs.acc[3], cs.A[1], cs.B[2]); SB(); conv_ldB<1,1>(cs); SB();
  mfma_a(cs.acc[3], cs.A[0], cs.B[3]); SB(); conv_ldB<2,2>(cs); SB();
  mfma_z(cs.acc[5], cs.A[23], cs.B[0]); SB(); conv_ldB<3,3>(cs); SB();
  mfma_z(cs.acc[4], cs.A[19], cs.B[0]); SB(); conv_ldA<16,1>(cs); conv_hand<3>(cs); SB();
  mfma_a(cs.acc[5], cs.A[22], cs.B[1]); SB(); conv_ldB<0,4>(cs); SB();
  mfma_a(cs.acc[4], cs.A[18], cs.B[1]); SB(); SB();
  mfma_a(cs.acc[5], cs.A[21], cs.B[2]); SB(); conv_ldB<1,5>(cs); SB();
  mfma_a(cs.acc[4], cs.A[17], cs.B[2]); SB(); SB();
  mfma_a(cs.acc[5], cs.A[20], cs.B[3]); SB(); conv_ldB<2,6>(cs); SB();
  mfma_a(cs.acc[4], cs.A[16], cs.B[3]); SB(); SB();
  mfma_a(cs.acc[5], cs.A[19], cs.B[0]); SB(); conv_ldB<3,7>(cs); SB();
  mfma_a(cs.acc[4], cs.A[15], cs.B[0]); SB(); SB();
  mfma_a(cs.acc[5], cs.A[18], cs.B[1]); SB(); conv_ldB<0,8>(cs); SB();
  mfma_a(cs.acc[4], cs.A[14], cs.B[1]); SB(); SB();
  mfma_a(cs.acc[5], cs.A[17], cs.B[2]); SB(); conv_ldB<1,9>(cs); SB();
  mfma_a(cs.acc[4], cs.A[13], cs.B[2]); SB(); SB();
  mfma_a(cs.acc[5], cs.A[16], cs.B[3]); SB(); conv_ldB<2,10>(cs); SB();
  mfma_a(cs.acc[4], cs.A[12], cs.B[3]); SB(); conv_ldA<27,0>(cs); SB();
  mfma_a(cs.acc[5], cs.A[15], cs.B[0]); SB(); conv_ldB<3,11>(cs); SB();
  mfma_a(cs.acc[4], cs.A[11], cs.B[0]); SB(); conv_ldA<27,1>(cs); SB();
  mfma_a(cs.acc[5], cs.A[14], cs.B[1]); SB(); conv_ldB<0,12>(cs); SB();
  mfma_a(cs.acc[4], cs.A[10], cs.B[1]); SB(); conv_ldA<30,0>(cs); SB();
  mfma_a(cs.acc[5], cs.A[13], cs.B[2]); SB(); conv_gpre(cs); conv_ldB<1,13>(cs); SB();
  mfma_a(cs.acc[4], cs.A[9], cs.B[2]); SB(); conv_ldA<30,1>(cs); SB();
  mfma_a(cs.acc[5], cs.A[12], cs.B[3]); SB(); conv_ldB<2,14>(cs); SB();
  mfma_a(cs.acc[4], cs.A[8], cs.B[3]); SB(); conv_ldA<26,0>(cs); SB();
  mfma_a(cs.acc[5], cs.A[11], cs.B[0]); SB(); conv_ldB<3,15>(cs); SB();
  mfma_a(cs.acc[4], cs.A[7], cs.B[0]); SB(); conv_ldA<26,1>(cs); SB();
  mfma_a(cs.acc[5], cs.A[10], cs.B[1]); SB(); conv_gate<0>(cs); conv_ldB<0,16>(cs); SB();
  mfma_a(cs.acc[4], cs.A[6], cs.B[1]); SB(); conv_ldA<29,0>(cs); SB();
  mfma_a(cs.acc[5], cs.A[9], cs.B[2]); SB(); conv_ldB<1,17>(cs); SB();
  mfma_a(cs.acc[4], cs.A[5], cs.B[2]); SB(); conv_ldA<29,1>(cs); SB();
  mfma_a(cs.acc[5], cs.A[8], cs.B[3]); SB(); conv_ldB<2,18>(cs); SB();
  mfma_a(cs.acc[4], cs.A[4], cs.B[3]); SB(); conv_ldA<25,0>(cs); SB();
  mfma_a(cs.acc[5], cs.A[7], cs.B[0]); SB(); conv_ldB<3,19>(cs); SB();
  mfma_a(cs.acc[4], cs.A[3], cs.B[0]); SB(); conv_ldA<25,1>(cs); SB();
  mfma_a(cs.acc[5], cs.A[6], cs.B[1]); SB(); conv_ldB<0,20>(cs); SB();
  mfma_a(cs.acc[4], cs.A[2], cs.B[1]); SB(); conv_ldA<28,0>(cs); SB();
  mfma_a(cs.acc[5], cs.A[5], cs.B[2]); SB(); conv_ldB<1,21>(cs); SB();
  mfma_a(cs.acc[4], cs.A[1], cs.B[2]); SB(); conv_ldA<28,1>(cs); SB();
  mfma_a(cs.acc[5], cs.A[4], cs.B[3]); SB(); conv_ldB<2,22>(cs); conv_ldA<31,0>(cs); SB();
  mfma_a(cs.acc[4], cs.A[0], cs.B[3]); SB(); conv_ldA<31,1>(cs); conv_ldA<24,0>(cs); SB();
  mfma_a(cs.acc[5], cs.A[3], cs.B[0]); SB(); conv_ldB<3,23>(cs); SB();
  mfma_a(cs.acc[5], cs.A[2], cs.B[1]); SB(); conv_ldB<0,0>(cs); conv_hand<4>(cs); SB();
  mfma_a(cs.acc[5], cs.A[1], cs.B[2]); SB(); conv_ldB<1,1>(cs); SB();
  mfma_a(cs.acc[5], cs.A[0], cs.B[3]); SB(); conv_ldB<2,2>(cs); SB();
  mfma_z(cs.acc[7], cs.A[31], cs.B[0]); SB(); conv_ldB<3,3>(cs); SB();
  mfma_z(cs.acc[6], cs.A[27], cs.B[0]); SB(); conv_ldA<24,1>(cs); conv_hand<5>(cs); SB();
  mfma_a(cs.acc[7], cs.A[30], cs.B[1]); SB(); conv_ldB<0,4>(cs); SB();
  mfma_a(cs.acc[6], cs.A[26], cs.B[1]); SB(); SB();
  mfma_a(cs.acc[7], cs.A[29], cs.B[2]); SB(); conv_ldB<1,5>(cs); SB();
  mfma_a(cs.acc[6], cs.A[25], cs.B[2]); SB(); SB();
  mfma_a(cs.acc[7], cs.A[28], cs.B[3]); SB(); conv_ldB<2,6>(cs); SB();
  mfma_a(cs.acc[6], cs.A[24], cs.B[3]); SB(); SB();
  mfma_a(cs.acc[7], cs.A[27], cs.B[0]); SB(); conv_ldB<3,7>(cs); SB();
  mfma_a(cs.acc[6], cs.A[23], cs.B[0]); SB(); SB();
  mfma_a(cs.acc[7], cs.A[26], cs.B[1]); SB(); conv_ldB<0,8>(cs); SB();
  mfma_a(cs.acc[6], cs.A[22], cs.B[1]); SB(); SB();
  mfma_a(cs.acc[7], cs.A[25], cs.B[2]); SB(); conv_ldB<1,9>(cs); SB();
  mfma_a(cs.acc[6], cs.A[21], cs.B[2]); SB(); SB();
  mfma_a(cs.acc[7], cs.A[24], cs.B[3]); SB(); conv_ldB<2,10>(cs); SB();
  mfma_a(cs.acc[6], cs.A[20], cs.B[3]); SB(); SB();
  mfma_a(cs.acc[7], cs.A[23], cs.B[0]); SB(); conv_ldB<3,11>(cs); SB();
  mfma_a(cs.acc[6], cs.A[19], cs.B[0]); SB(); SB();
  mfma_a(cs.acc[7], cs.A[22], cs.B[1]); SB(); conv_ldB<0,12>(cs); SB();
  mfma_a(cs.acc[6], cs.A[18], cs.B[1]); SB(); SB();
  mfma_a(cs.acc[7], cs.A[21], cs.B[2]); SB(); conv_ldB<1,13>(cs); SB();
  mfma_a(cs.acc[6], cs.A[17], cs.B[2]); SB(); SB();
  mfma_a(cs.acc[7], cs.A[20], cs.B[3]); SB(); conv_ldB<2,14>(cs); SB();
  mfma_a(cs.acc[6], cs.A[16], cs.B[3]); SB(); SB();
  mfma_a(cs.acc[7], cs.A[19], cs.B[0]); SB(); conv_ldB<3,15>(cs); SB();
  mfma_a(cs.acc[6], cs.A[15], cs.B[0]); SB(); SB();
  mfma_a(cs.acc[7], cs.A[18], cs.B[1]); SB(); conv_ldB<0,16>(cs); SB();
  mfma_a(cs.acc[6], cs.A[14], cs.B[1]); SB(); SB();
  mfma_a(cs.acc[7], cs.A[17], cs.B[2]); SB(); conv_ldB<1,17>(cs); SB();
  mfma_a(cs.acc[6], cs.A[13], cs.B[2]); SB(); SB();
  mfma_a(cs.acc[7], cs.A[16], cs.B[3]); SB(); conv_ldB<2,18>(cs); SB();
  mfma_a(cs.acc[6], cs.A[12], cs.B[3]); SB(); SB();
  mfma_a(cs.acc[7], cs.A[15], cs.B[0]); SB(); conv_ldB<3,19>(cs); SB();
  mfma_a(cs.acc[6], cs.A[11], cs.B[0]); SB(); SB();
  mfma_a(cs.acc[7], cs.A[14], cs.B[1]); SB(); conv_ldB<0,20>(cs); SB();
  mfma_a(cs.acc[6], cs.A[10], cs.B[1]); SB(); SB();
  mfma_a(cs.acc[7], cs.A[13], cs.B[2]); SB(); conv_gpre(cs); conv_ldB<1,21>(cs); SB();
  mfma_a(cs.acc[6], cs.A[9], cs.B[2]); SB(); SB();
  mfma_a(cs.acc[7], cs.A[12], cs.B[3]); SB(); conv_ldB<2,22>(cs); SB();
  mfma_a(cs.acc[6], cs.A[8], cs.B[3]); SB(); SB();
  mfma_a(cs.acc[7], cs.A[11], cs.B[0]); SB(); conv_ldB<3,23>(cs); SB();
  mfma_a(cs.acc[6], cs.A[7], cs.B[0]); SB(); SB();
  mfma_a(cs.acc[7], cs.A[10], cs.B[1]); SB(); conv_gate<1>(cs); conv_ldB<0,24>(cs); SB();
  mfma_a(cs.acc[6], cs.A[6], cs.B[1]); SB(); SB();
  mfma_a(cs.acc[7], cs.A[9], cs.B[2]); SB(); conv_ldB<1,25>(cs); SB();
  mfma_a(cs.acc[6], cs.A[5], cs.B[2]); SB(); SB();
  mfma_a(cs.acc[7], cs.A[8], cs.B[3]); SB(); conv_ldB<2,26>(cs); SB();
  mfma_a(cs.acc[6], cs.A[4], cs.B[3]); SB(); SB();
  mfma_a(cs.acc[7], cs.A[7], cs.B[0]); SB(); conv_ldB<3,27>(cs); SB();
  mfma_a(cs.acc[6], cs.A[3], cs.B[0]); SB(); SB();
  mfma_a(cs.acc[7], cs.A[6], cs.B[1]); SB(); conv_ldB<0,28>(cs); SB();
  mfma_a(cs.acc[6], cs.A[2], cs.B[1]); SB(); SB();
  mfma_a(cs.acc[7], cs.A[5], cs.B[2]); SB(); conv_ldB<1,29>(cs); SB();
  mfma_a(cs.acc[6], cs.A[1], cs.B[2]); SB(); SB();
  mfma_a(cs.acc[7], cs.A[4], cs.B[3]); SB(); conv_ldB<2,30>(cs); SB();
  mfma_a(cs.acc[6], cs.A[0], cs.B[3]); SB(); SB();
  mfma_a(cs.acc[7], cs.A[3], cs.B[0]); SB(); conv_ldB<3,31>(cs); SB();
  mfma_a(cs.acc[7], cs.A[2], cs.B[1]); SB(); conv_hand<6>(cs); SB();
  mfma_a(cs.acc[7], cs.A[1], cs.B[2]); SB(); SB();
  mfma_a(cs.acc[7], cs.A[0], cs.B[3]); SB(); SB();
  asm volatile("s_nop 7\n\ts_nop 3" ::: "memory"); conv_hand<7>(cs);
}

__global__ __launch_bounds__(640, 3) void ssm_conv_s4d(const uint16_t* __restrict__ u, const uint16_t* __restrict__ logA,
                                                       const uint16_t* __restrict__ Aim, const uint16_t* __restrict__ Cre,
                                                       const uint16_t* __restrict__ Cim, const uint16_t* __restrict__ logdt,
                                                       const uint16_t* __restrict__ Dsk, uint16_t* __restrict__ out,
                                                       char* ws) {
  __shared__ __attribute__((aligned(16))) char smem[SMEM];
  LDS char* sm = (LDS char*)smem;
  LDS uint32_t* cnt = (LDS uint32_t*)(sm + OFF_CNT);
  const int slot = blockIdx.x & 7, k = blockIdx.x >> 3;
  const int x = (slot < 4) ? slot : (11 - slot);
  const int lane = threadIdx.x & 63;
  if (x == 7) {
    const __amdgpu_buffer_rsrc_t wsr = __builtin_amdgcn_make_buffer_rsrc(ws, 0, WS_BYTES, 0x00020000);
    const int v = __builtin_amdgcn_readfirstlane(threadIdx.x >> 6);
    const int task = 4 * k + v;
    if (v >= 4 || task >= 112) return;
    const int rb = task / 7, g = task - 7 * rb;
    const unsigned long long t0 = __builtin_amdgcn_s_memrealtime();
    const uint32_t uo = (uint32_t)(((32 * rb + 4 * (lane & 7)) * DD + 64 * g + 8 * (lane >> 3)) * 2);
    const uint32_t go = WS_YT + ((64 * g + 8 * (lane >> 3)) * 256 + 8 * rb + (lane & 7)) * 16;
    const uint32_t ep = __builtin_amdgcn_raw_buffer_load_b32(wsr, WS_EP + g * 256, 0, SC1);
    u4 wv[4];
#pragma unroll
    for (int i = 0; i < 4; i++) wv[i] = *(const u4*)((const char*)u + uo + i * DD * 2);
    const u4 dsk = *(const u4*)(Dsk + 64 * g + 8 * (lane >> 3));
    uint32_t tY, tO;
    asm volatile("global_load_dword %0, %2, %3\n\tglobal_load_dword %1, %4, %5" : "=&v"(tY), "=&v"(tO) : "v"(go), "s"(ws), "v"(uo), "s"(out));
    const uint32_t tag = make_tag(__builtin_amdgcn_readfirstlane(ep));
    if (lane == 0) __builtin_amdgcn_raw_buffer_store_b128((u4){tag, tag, tag, tag}, wsr, WS_ACK + (g * 64 + 32 + rb) * 16, 0, SC1);
    f2v sk[4][4];
#pragma unroll
    for (int i = 0; i < 4; i++)
#pragma unroll
      for (int e = 0; e < 4; e++) {
        const uint32_t p = pkbf(bflo(wv[i][e]) * bflo(dsk[e]), bfhi(wv[i][e]) * bfhi(dsk[e]));
        sk[i][e] = (f2v){bflo(p), bfhi(p)};
        asm volatile("" : "+v"(sk[i][e]));
      }
    asm volatile("s_waitcnt vmcnt(0)" :: "v"(tY), "v"(tO) : "memory");
    while (__builtin_amdgcn_s_memrealtime() - t0 < TGATE) __builtin_amdgcn_s_sleep(8);
    for (int spins = 0; spins < SPIN_MAX; spins++) {
      asm volatile("" ::: "memory");
      u4 g7 = __builtin_amdgcn_raw_buffer_load_b128(wsr, go + 7 * 256 * 16, 0, SC1);
      if (__all((g7[1] == tag) & (g7[3] == tag))) break;
      __builtin_amdgcn_s_sleep(PSL);
    }
    u4 gr[8];
    for (int spins = 0; spins < SPIN_MAX; spins++) {
      asm volatile("" ::: "memory");
      bool ok = true;
#pragma unroll
      for (int e = 0; e < 8; e++) gr[e] = __builtin_amdgcn_raw_buffer_load_b128(wsr, go + e * 256 * 16, 0, SC1);
#pragma unroll
      for (int e = 0; e < 8; e++) ok = ok & (gr[e][1] == tag) & (gr[e][3] == tag);
      if (__all(ok)) break;
      __builtin_amdgcn_s_sleep(PSL);
    }
    const __amdgpu_buffer_rsrc_t outr = __builtin_amdgcn_make_buffer_rsrc(out, 0, LL * DD * 2, 0x00020000);
#pragma unroll
    for (int i = 0; i < 4; i++) {
      u4 o;
#pragma unroll
      for (int e = 0; e < 4; e++) {
        const uint32_t da = gr[2 * e][(i >> 1) * 2], db = gr[2 * e + 1][(i >> 1) * 2];
        f2v y = (i & 1) ? (f2v){bfhi(da), bfhi(db)} : (f2v){bflo(da), bflo(db)};
        y = y + sk[i][e];
        o[e] = pkbf(y.x, y.y);
      }
      __builtin_amdgcn_raw_buffer_store_b128(o, outr, uo + i * DD * 2, 0, SC1);
    }
    return;
  }
  const int c0 = 64 * x + 2 * k;
  const int tid = threadIdx.x;
  const int w = __builtin_amdgcn_readfirstlane(tid >> 6);
  if (w >= 8) {
    const int j = w - 8;
    const __amdgpu_buffer_rsrc_t wsr = __builtin_amdgcn_make_buffer_rsrc(ws, 0, WS_BYTES, 0x00020000);
    __builtin_amdgcn_s_setprio(3);
    if (lane < 32) *(LDS u4*)(sm + OFF_UT + j * UT_CH + lane * 16) = (u4){0, 0, 0, 0};
    __builtin_amdgcn_s_barrier();
    u4 wv0, wv1, wv2, wv3;
    uint32_t touch, ep;
    {
      const uint32_t o = (uint32_t)(((64 * k + 32 * j + 4 * (lane & 7)) * DD + 64 * x + 8 * (lane >> 3)) * 2);
      asm volatile("global_load_dwordx4 %0, %5, %6\n\tglobal_load_dwordx4 %1, %5, %6 offset:896\n\t"
                   "global_load_dwordx4 %2, %5, %6 offset:1792\n\tglobal_load_dwordx4 %3, %5, %6 offset:2688\n\t"
                   "global_load_dword %4, %5, %7"
                   : "=&v"(wv0), "=&v"(wv1), "=&v"(wv2), "=&v"(wv3), "=&v"(touch) : "v"(o), "s"(u), "s"(out));
      asm volatile("global_load_dword %0, %1, %2 sc1" : "=v"(ep) : "v"((uint32_t)(WS_EP + x * 256)), "s"(ws));
    }
    uint32_t ul[8];
    const uint32_t vo = (uint32_t)(((64 * j + lane) * DD + c0) * 2);
#pragma unroll
    for (int i = 0; i < 8; i++)
      asm volatile("global_load_dword %0, %1, %2" : "=v"(ul[i]) : "v"(vo), "s"(u + (size_t)128 * i * DD));
    LDS uint16_t* ut = (LDS uint16_t*)(sm + OFF_UT) + 256 + 64 * j + lane;
    asm volatile("s_waitcnt vmcnt(8)" : "+v"(wv0), "+v"(wv1), "+v"(wv2), "+v"(wv3), "+v"(touch), "+v"(ep) :: "memory");
    const uint32_t tag = make_tag(__builtin_amdgcn_readfirstlane(ep));
    if (j == 0) flag_set(cnt + FL_TAG, tag);
    if (k >= 16) {
      const uint32_t po = WS_UT + ((64 * x + 8 * (lane >> 3)) * 256 + 16 * (k - 16) + 8 * j + (lane & 7)) * 16;
#pragma unroll
      for (int e = 0; e < 4; e++) {
        u4 g0 = {PERM_LO(wv0[e], wv1[e]), tag, PERM_LO(wv2[e], wv3[e]), tag};
        u4 g1 = {PERM_HI(wv0[e], wv1[e]), tag, PERM_HI(wv2[e], wv3[e]), tag};
        __builtin_amdgcn_raw_buffer_store_b128(g0, wsr, po + (2 * e) * 256 * 16, 0, SC1);
        __builtin_amdgcn_raw_buffer_store_b128(g1, wsr, po + (2 * e + 1) * 256 * 16, 0, SC1);
      }
    }
    asm volatile("" ::: "memory");
#define ULDS(i) ut[128 * i] = (uint16_t)ul[i]; ut[UT_CH / 2 + 128 * i] = (uint16_t)(ul[i] >> 16);
    if (k >= 16) asm volatile("s_waitcnt vmcnt(12)" ::: "memory"); else asm volatile("s_waitcnt vmcnt(4)" ::: "memory");
    asm volatile("" : "+v"(ul[0]), "+v"(ul[1]), "+v"(ul[2]), "+v"(ul[3]));
    ULDS(0) ULDS(1) ULDS(2) ULDS(3)
    BAR();
    if (j == 0) {
      if (lane == 0) __builtin_amdgcn_raw_buffer_store_b128((u4){tag, tag, tag, tag}, wsr, WS_ACK + (x * 64 + k) * 16, 0, SC1);
      asm volatile("" ::: "memory");
      if (k >= 16) asm volatile("s_waitcnt vmcnt(9)" ::: "memory"); else asm volatile("s_waitcnt vmcnt(1)" ::: "memory");
    } else {
      if (k >= 16) asm volatile("s_waitcnt vmcnt(8)" ::: "memory"); else asm volatile("s_waitcnt vmcnt(0)" ::: "memory");
    }
    asm volatile("" : "+v"(ul[4]), "+v"(ul[5]), "+v"(ul[6]), "+v"(ul[7]));
    ULDS(4) ULDS(5) ULDS(6) ULDS(7)
    BAR();
    {
      const uint32_t go = WS_UT + ((c0 + j) * 256 + lane) * 16;
      u4 g0, g1, g2, g3;
      for (int spins = 0; spins < SPIN_MAX; spins++) {
        asm volatile("" ::: "memory");
        g0 = __builtin_amdgcn_raw_buffer_load_b128(wsr, go, 0, SC1);
        g1 = __builtin_amdgcn_raw_buffer_load_b128(wsr, go + 64 * 16, 0, SC1);
        g2 = __builtin_amdgcn_raw_buffer_load_b128(wsr, go + 128 * 16, 0, SC1);
        g3 = __builtin_amdgcn_raw_buffer_load_b128(wsr, go + 192 * 16, 0, SC1);
        bool ok = (g0[1] == tag) & (g0[3] == tag) & (g1[1] == tag) & (g1[3] == tag) &
                  (g2[1] == tag) & (g2[3] == tag) & (g3[1] == tag) & (g3[3] == tag);
        if (__all(ok)) break;
      }
      LDS char* ug = sm + OFF_UT + j * UT_CH + (256 + 1024) * 2 + lane * 8;
      *(LDS u2*)(ug) = (u2){g0[0], g0[2]};
      *(LDS u2*)(ug + 512) = (u2){g1[0], g1[2]};
      flag_set(cnt + FL_U + j);
      *(LDS u2*)(ug + 1024) = (u2){g2[0], g2[2]};
      *(LDS u2*)(ug + 1536) = (u2){g3[0], g3[2]};
      flag_set(cnt + FL_U + 2 + j);
    }
    if (k == 31 && j == 0) {
      for (int spins = 0; spins < SPIN_MAX; spins++) {
        asm volatile("" ::: "memory");
        u4 a = __builtin_amdgcn_raw_buffer_load_b128(wsr, WS_ACK + (x * 64 + min(lane, 47)) * 16, 0, SC1);
        if (__all((a[0] == tag) & (a[1] == tag) & (a[2] == tag) & (a[3] == tag))) break;
      }
      if (lane == 0) __builtin_amdgcn_raw_buffer_store_b32(tag >> 1, wsr, WS_EP + x * 256, 0, SC1);
    }
    return;
  }
  const int ch = w >> 2;
  const int c = c0 + ch;
  const int n = (w & 3) * 64 + lane;
  uint32_t rA = logA[c * NN + n], rI = Aim[c * NN + n], rCr = Cre[c * NN + n], rCi = Cim[c * NN + n];
  uint32_t rdt = logdt[c];
  uint32_t dn2;
  asm volatile("global_load_dword %0, %1, %2" : "=v"(dn2) : "v"((uint32_t)(c0 * 2)), "s"(Dsk));
  uint32_t tws;
  asm volatile("global_load_dword %0, %1, %2" : "=v"(tws) : "v"((uint32_t)(WS_EP + x * 256)), "s"(ws));
  SB();
  const int ht_ = tid & 255;
  if (tid < 64) cnt[tid] = 0;
  BAR();
  const uint32_t wb = (uint32_t)w * 256u;
  uint32_t pbv = (uint32_t)(uintptr_t)(sm + OFF_TAB) + (lane & 31) * TROW + w * 256 + (lane >> 5) * 16;
  uint32_t aM, aX, aX2, aK, pav;
  const int ip = 255 - ht_;
  const int er = ip & 31;
  const int wq = w & 3;
  {
    const uint32_t pw = (uint32_t)(uintptr_t)(sm + OFF_TAB) + ch * 1024;
    const uint32_t pr = pw + er * TROW;
    const int beta3 = (ip - 128) >> 5, beta2 = (ip - 64) >> 5;
    const bool r1 = ip >= 32;
    int cM, cN, cW;
    if (wq < 2) { cM = 16; cN = 16 + beta3; cW = 17 + beta3; }
    else if (wq == 2) { cM = 12; cN = 12 + 2 * beta2; cW = 14 + 2 * beta2; }
    else { cM = r1 ? 8 : 0; cN = 8; cW = 12; }
    aM = pr + cM * 4;
    aX = (er == 31) ? pw + cW * 4 : pr + TROW + cN * 4;
    aX2 = pw + ((er == 0) ? 31 : er - 1) * TROW + 7 * 4;
    aK = (uint32_t)(uintptr_t)(sm + OFF_KR) + ch * KR_CH + ht_ * 16;
    pav = pbv + 32 * TROW;
    asm volatile("" : "+v"(aM), "+v"(aX), "+v"(aX2), "+v"(aK), "+v"(pbv), "+v"(pav));
  }
  uint32_t cub, cka, cyx;
  {
    const int cch = (w >> 1) & 1, par = w & 1;
    const int bb = lane & 15, q = lane >> 4;
    cub = (uint32_t)(uintptr_t)(sm + OFF_UT + cch * UT_CH + (256 + 32 * (-6 - par) - 16 + 16 * bb + 8 * q) * 2);
    const int x0 = 2031 - 32 * par - bb + 8 * q;
    cka = (uint32_t)(uintptr_t)(sm + OFF_KR + cch * KR_CH + ((bb & 1) ? 2 * x0 : (KR_COPY + 2 * (x0 - 1))) - 31 * 128);
    cyx = (uint32_t)(uintptr_t)(sm + OFF_YX + (cch * 2 + par) * 1024 + (16 * q + ((bb + 4 * q) & 15)) * 16);
    asm volatile("" : "+v"(cub), "+v"(cka), "+v"(cyx));
  }
  tab_base(wb);
  { h2 e0 = {(_Float16)1.0f, (_Float16)0.0f}; tw<0, 0>(e0); }
  if (ht_ < 8) *(LDS u4*)(sm + OFF_KR + ch * KR_CH + (ht_ >> 2) * KR_COPY + 4096 + (ht_ & 3) * 16) = (u4){0, 0, 0, 0};
  LDS char* pb = (LDS char*)(uintptr_t)pbv;
  asm volatile("s_waitcnt vmcnt(0)" : "+v"(rA), "+v"(rI), "+v"(rCr), "+v"(rCi), "+v"(rdt), "+v"(dn2), "+v"(tws) :: "memory");
  SB();
  float dt, ar2, ai;
  {
    const f2v xv = {bf2f(rdt), bf2f(rA)};
    const f2v H = {1.44269502162933349609375f, 1.44269502162933349609375f}, Lo = {1.925963033500011e-08f, 1.925963033500011e-08f};
    f2v t = xv * H;
    f2v e = __builtin_elementwise_fma(xv, H, -t);
    e = __builtin_elementwise_fma(xv, Lo, e);
    f2v r = {__builtin_amdgcn_exp2f(t.x), __builtin_amdgcn_exp2f(t.y)};
    e = e * 0.693147180559945309f;
    r = __builtin_elementwise_fma(r, e, r);
    const uint32_t pk = pkbf(r.x, r.y);
    dt = bflo(pk);
    const float ar = -bfhi(pk) * dt;
    ai = bf2f(rI) * dt;
    ar2 = ar * 1.44269504088896340736f;
  }
  c32x2 za;
  {
    const f2v P = {1.0f, 128.0f};
    const f2v HI = {0.15915493667125701904296875f, 0.15915493667125701904296875f}, LO = {6.4206382432e-09f, 6.4206382432e-09f};
    f2v me = P * ar2;
    f2v xx = P * ai;
    f2v h = xx * HI;
    f2v e = __builtin_elementwise_fma(xx, HI, -h);
    e = __builtin_elementwise_fma(xx, LO, e);
    f2v fr = {__builtin_amdgcn_fractf(h.x), __builtin_amdgcn_fractf(h.y)};
    fr = fr + e;
    f2v m = {__builtin_amdgcn_exp2f(me.x), __builtin_amdgcn_exp2f(me.y)};
    f2v cs = {__builtin_amdgcn_cosf(fr.x), __builtin_amdgcn_cosf(fr.y)};
    f2v sn = {__builtin_amdgcn_sinf(fr.x), __builtin_amdgcn_sinf(fr.y)};
    za.r = m * cs; za.i = m * sn;
  }
  const c32x2 zb = csq2(za), zc = csq2(zb), zd = csq2(zc);
  const c32 z1 = {za.r.x, za.i.x}, z128 = {za.r.y, za.i.y}, z2 = {zb.r.x, zb.i.x}, z256 = {zb.r.y, zb.i.y};
  const c32 z4 = {zc.r.x, zc.i.x}, z512 = {zc.r.y, zc.i.y}, z8 = {zd.r.x, zd.i.x}, z1024 = {zd.r.y, zd.i.y};
  const c32 z16 = csq(z8), z32 = csq(z16), z64 = csq(z32);
  h2 e1 = tohc(z8), e2 = tohc(z16), e4 = tohc(z32), e8 = tohc(z64), e16 = tohc(z128);
  h8 bv[8];
  float ks;
  {
    tw<0, 1>(e1); tw<0, 2>(e2); tw<0, 4>(e4); tw<0, 8>(e8); tw<0, 16>(e16);
    h2 e3 = cmul16(e1, e2); tw<0, 3>(e3);
    h2 e5 = cmul16(e1, e4); tw<0, 5>(e5);
    h2 e6 = cmul16(e2, e4); tw<0, 6>(e6);
    h2 e7 = cmul16(e3, e4); tw<0, 7>(e7);
    h2 e9 = cmul16(e1, e8); tw<0, 9>(e9);
    h2 e10 = cmul16(e2, e8); tw<0, 10>(e10);
    h2 e11 = cmul16(e3, e8); tw<0, 11>(e11);
    h2 e12 = cmul16(e4, e8); tw<0, 12>(e12);
    h2 e13 = cmul16(e5, e8); tw<0, 13>(e13);
    h2 e14 = cmul16(e6, e8); tw<0, 14>(e14);
    h2 e15 = cmul16(e7, e8); tw<0, 15>(e15);
    {
      h2 t0 = cmul16(e1, e16);
      h2 t1 = cmul16(e2, e16);
      h2 t2 = cmul16(e3, e16);
      h2 t3 = cmul16(e4, e16);
      SB(); tw<0, 17>(t0); tw<0, 18>(t1); SB();
      h2 t4 = cmul16(e5, e16);
      h2 t5 = cmul16(e6, e16);
      asm volatile("" :: "v"(t4), "v"(t5), "v"(t0), "v"(t1));
      SB(); tw<0, 19>(t2); tw<0, 20>(t3); SB();
      h2 t6 = cmul16(e7, e16);
      h2 t7 = cmul16(e8, e16);
      asm volatile("" :: "v"(t6), "v"(t7), "v"(t2), "v"(t3));
      SB(); tw<0, 21>(t4); tw<0, 22>(t5); SB();
      h2 t8 = cmul16(e9, e16);
      h2 t9 = cmul16(e10, e16);
      asm volatile("" :: "v"(t8), "v"(t9), "v"(t4), "v"(t5));
      SB(); tw<0, 23>(t6); tw<0, 24>(t7); SB();
      h2 t10 = cmul16(e11, e16);
      h2 t11 = cmul16(e12, e16);
      asm volatile("" :: "v"(t10), "v"(t11), "v"(t6), "v"(t7));
      SB(); tw<0, 25>(t8); tw<0, 26>(t9); SB();
      h2 t12 = cmul16(e13, e16);
      h2 t13 = cmul16(e14, e16);
      asm volatile("" :: "v"(t12), "v"(t13), "v"(t8), "v"(t9));
      SB(); tw<0, 27>(t10); tw<0, 28>(t11); SB();
      h2 t14 = cmul16(e15, e16);
      asm volatile("" :: "v"(t14), "v"(t10), "v"(t11));
      SB(); tw<0, 29>(t12); tw<0, 30>(t13); SB();
      tw<0, 31>(t14);
    }
#pragma unroll
    for (int s = 0; s < 8; s++) bv[s] = *(LDS h8*)(pb + s * 32);
    tab_base(wb + 49152);
    uint32_t mx = max(rCr & 0x7FFFu, rCi & 0x7FFFu);
    uint32_t eb = wave_max_u32(mx) >> 7;
    eb = eb < 1 ? 127 : (eb > 253 ? 253 : eb);
    const float sc = __uint_as_float((254u - eb) << 23);
    ks = 2.0f * dt * __uint_as_float(eb << 23);
    c32 C; C.r = bf2f(rCr) * sc; C.i = bf2f(rCi) * sc;
    h2 y1 = toh(z1), y2 = toh(z2), y4 = toh(z4), y256 = toh(z256), y512 = toh(z512), y1024 = toh(z1024);
    h2 g0 = toh(C); tw<49152, 32>(g0);
    h2 g1 = cmul16(g0, y1); tw<49152, 33>(g1);
    h2 g2 = cmul16(g0, y2); tw<49152, 34>(g2);
    h2 g4 = cmul16(g0, y4); tw<49152, 36>(g4);
    h2 a0 = cmul16(g0, y256); tw<49152, 40>(a0);
    h2 b0 = cmul16(g0, y512); tw<49152, 44>(b0);
    h2 f0 = cmul16(g0, y1024); tw<49152, 48>(f0);
    h2 g3 = cmul16(g1, y2); tw<49152, 35>(g3);
    h2 g6 = cmul16(g2, y4);
    h2 a4 = cmul16(g4, y256);
    h2 d0 = cmul16(a0, y512);
    tw<49152, 38>(g6); tw<49152, 42>(a4); tw<49152, 46>(d0);
    {
      h2 t0 = cmul16(g1, y4);
      h2 t1 = cmul16(g2, y256);
      h2 t2 = cmul16(g4, y512);
      h2 t3 = cmul16(a0, y1024);
      SB(); tw<49152, 37>(t0); tw<49152, 41>(t1); SB();
      h2 t4 = cmul16(b0, y1024);
      h2 t5 = cmul16(f0, y1024);
      asm volatile("" :: "v"(t4), "v"(t5), "v"(t0), "v"(t1));
      SB(); tw<49152, 45>(t2); tw<49152, 49>(t3); SB();
      h2 t6 = cmul16(g3, y4);
      h2 t7 = cmul16(g6, y256);
      asm volatile("" :: "v"(t6), "v"(t7), "v"(t2), "v"(t3));
      SB(); tw<49152, 50>(t4); tw<49152, 52>(t5); SB();
      h2 t8 = cmul16(a4, y512);
      h2 t9 = cmul16(d0, y1024);
      asm volatile("" :: "v"(t8), "v"(t9), "v"(t4), "v"(t5));
      SB(); tw<49152, 39>(t6); tw<49152, 43>(t7); SB();
      tw<49152, 47>(t8); tw<49152, 51>(t9);
    }
  }
  {
    LDS char* pa = (LDS char*)(uintptr_t)pav;
    h8 av[8];
#pragma unroll
    for (int s = 0; s < 8; s++) av[s] = *(LDS h8*)(pa + s * 32);
    SB();
    f16v acc = {0};
#pragma unroll
    for (int s = 0; s < 8; s++) acc = __builtin_amdgcn_mfma_f32_32x32x16_f16(av[s], bv[s], acc, 0, 0, 0);
#pragma unroll
    for (int g = 0; g < 3; g++) {
      f4 v = {acc[4 * g] * ks, acc[4 * g + 1] * ks, acc[4 * g + 2] * ks, acc[4 * g + 3] * ks};
      *(LDS f4*)(pb + g * 32) = v;
    }
  }
  BAR();
  {
    LDS char* pM = (LDS char*)(uintptr_t)aM;
    LDS char* pX = (LDS char*)(uintptr_t)aX;
    float val[8], prev;
    if (wq == 3) {
      LDS char* pP = (LDS char*)(uintptr_t)aX2;
      f4 m0 = *(LDS f4*)pM, m1 = *(LDS f4*)(pM + 256), m2 = *(LDS f4*)(pM + 512), m3 = *(LDS f4*)(pM + 768);
      f4 n0 = *(LDS f4*)(pM + 16), n1 = *(LDS f4*)(pM + 16 + 256), n2 = *(LDS f4*)(pM + 16 + 512), n3 = *(LDS f4*)(pM + 16 + 768);
      float x0 = *(LDS float*)pX, x1 = *(LDS float*)(pX + 256), x2 = *(LDS float*)(pX + 512), x3 = *(LDS float*)(pX + 768);
      float p0 = *(LDS float*)pP, p1 = *(LDS float*)(pP + 256), p2 = *(LDS float*)(pP + 512), p3 = *(LDS float*)(pP + 768);
      SB();
      f4 q0 = (m0 + m1) + (m2 + m3), q1 = (n0 + n1) + (n2 + n3);
      float nx = (x0 + x1) + (x2 + x3), pv = (p0 + p1) + (p2 + p3);
      const bool r1 = ip >= 32;
      val[0] = q0[0];
      val[1] = r1 ? q0[0] : q0[1];
      val[2] = r1 ? q0[1] : q0[2];
      val[3] = r1 ? q0[2] : q0[3];
      val[4] = r1 ? q0[2] : q1[0];
      val[5] = r1 ? q0[2] : q1[1];
      val[6] = r1 ? q0[3] : q1[2];
      val[7] = r1 ? nx : q1[3];
      prev = r1 ? (er == 0 ? pv : q0[0]) : (er == 0 ? 0.0f : pv);
    } else {
      f4 m0 = *(LDS f4*)pM, m1 = *(LDS f4*)(pM + 256), m2 = *(LDS f4*)(pM + 512), m3 = *(LDS f4*)(pM + 768);
      float x0 = *(LDS float*)pX, x1 = *(LDS float*)(pX + 256), x2 = *(LDS float*)(pX + 512), x3 = *(LDS float*)(pX + 768);
      SB();
      f4 q = (m0 + m1) + (m2 + m3);
      float nx = (x0 + x1) + (x2 + x3);
      if (wq == 2) {
        const bool be = ip >= 96;
        float b0 = be ? q[2] : q[0], b1 = be ? q[3] : q[1];
        val[0] = val[1] = val[2] = b0; val[3] = val[4] = val[5] = b1; val[6] = val[7] = nx; prev = b0;
      } else {
        const int beta = (ip - 128) >> 5;
        float lo2 = (beta & 1) ? q[1] : q[0], hi2 = (beta & 1) ? q[3] : q[2];
        float d0 = (beta & 2) ? hi2 : lo2;
        float mid = (ip & 1) ? nx : d0;
        val[0] = val[1] = val[2] = val[3] = d0; val[4] = mid; val[5] = val[6] = val[7] = nx; prev = d0;
      }
    }
    u4 c0v = {pkbf(val[7], val[6]), pkbf(val[5], val[4]), pkbf(val[3], val[2]), pkbf(val[1], val[0])};
    u4 c1v = {pkbf(val[6], val[5]), pkbf(val[4], val[3]), pkbf(val[2], val[1]), pkbf(val[0], prev)};
    *(LDS u4*)(uintptr_t)(aK) = c0v;
    *(LDS u4*)(uintptr_t)(aK + KR_COPY) = c1v;
  }
  if (w < 4) {
    ConvState cs;
    cs.ub = cub; cs.ka[0] = cka; cs.ka[1] = cka + 1024; cs.ka[2] = cka + 2048; cs.ka[3] = cka + 3072; cs.yx = cyx;
    asm volatile("" : "+v"(cs.ka[0]), "+v"(cs.ka[1]), "+v"(cs.ka[2]), "+v"(cs.ka[3]), "+v"(cs.ub), "+v"(cs.yx));
    cs.cnt = cnt; cs.wid = w;
    conv_pre(cs);
    BAR();
    conv_run(cs);
  } else {
    const int h = w - 4, hc = h >> 1, ht = h & 1;
    const __amdgpu_buffer_rsrc_t wsr = __builtin_amdgcn_make_buffer_rsrc(ws, 0, WS_BYTES, 0x00020000);
    __builtin_amdgcn_s_setprio(3);
    const int rr = 64 * h + lane;
    const int aa = rr >> 4, qq = (rr >> 2) & 3, ii = rr & 3;
    LDS char* yp = sm + OFF_YX + (16 * qq + ((aa + 4 * qq) & 15)) * 16 + ii * 4;
    LDS char* up = sm + OFF_UT + (256 + rr) * 2;
    uint16_t* op = out + (size_t)rr * DD + c0;
    LDS char* yq = sm + OFF_YX + hc * 2048 + (16 * (lane & 3) + (((lane >> 2) + 4 * (lane & 3)) & 15)) * 16;
    const uint32_t yo = WS_YT + ((c0 + hc) * 256 + lane) * 16;
    asm volatile("s_waitcnt vmcnt(0)" : "+v"(dn2) :: "memory");
    const float d0 = bflo(dn2), d1 = bfhi(dn2);
    const uint32_t tag = __builtin_amdgcn_readfirstlane(*(volatile LDS uint32_t*)(cnt + FL_TAG));
    BAR();
    auto skip = [&](int t) -> uint32_t {
      uint32_t ua = *(LDS uint16_t*)(up + t * 512), ubb = *(LDS uint16_t*)(up + UT_CH + t * 512);
      return pkbf(bf2f(ua) * d0, bf2f(ubb) * d1);
    };
    auto tile = [&](int t, uint32_t sk) {
      flag_wait4(cnt + 4 * t);
      LDS char* y = yp + t * 4096;
      float y00 = *(LDS float*)(y), y01 = *(LDS float*)(y + 1024), y10 = *(LDS float*)(y + 2048), y11 = *(LDS float*)(y + 3072);
      uint32_t pa = pkbf(y00 + y01, y10 + y11);
      *(uint32_t*)(op + (size_t)t * 256 * DD) = pkbf(bflo(pa) + bflo(sk), bfhi(pa) + bfhi(sk));
    };
    uint32_t sk2 = skip(2), sk3 = skip(3);
    asm volatile("" : "+v"(sk2), "+v"(sk3));
    {
      flag_wait2(cnt + 4 * ht + 2 * hc);
      f4 v = *(LDS f4*)(yq + ht * 4096) + *(LDS f4*)(yq + ht * 4096 + 1024);
      __builtin_amdgcn_raw_buffer_store_b128((u4){pkbf(v[0], v[1]), tag, pkbf(v[2], v[3]), tag}, wsr, yo + ht * 64 * 16, 0, SC1);
    }
    tile(2, sk2);
    tile(3, sk3);
    flag_wait2(cnt + FL_U);
    uint32_t sk4 = skip(4), sk5 = skip(5);
    asm volatile("" : "+v"(sk4), "+v"(sk5));
    tile(4, sk4);
    tile(5, sk5);
    flag_wait2(cnt + FL_U + 2);
    uint32_t sk6 = skip(6), sk7 = skip(7);
    asm volatile("" : "+v"(sk6), "+v"(sk7));
    tile(6, sk6);
    tile(7, sk7);
  }
}

extern "C" void kernel_launch(void* const* d_in, const int* in_sizes, int n_in, void* d_out, int out_size,
                              void* d_ws, size_t ws_size, hipStream_t stream) {
  (void)in_sizes; (void)n_in; (void)out_size; (void)ws_size;
  ssm_conv_s4d<<<256, 640, 0, stream>>>((const uint16_t*)d_in[0], (const uint16_t*)d_in[1], (const uint16_t*)d_in[2],
                                        (const uint16_t*)d_in[3], (const uint16_t*)d_in[4], (const uint16_t*)d_in[5],
                                        (const uint16_t*)d_in[6], (uint16_t*)d_out, (char*)d_ws);
}
```
